```python
import torch

def _softmax(x, axis=-1):
    return torch.softmax(x, dim=axis)
def _gelu(x):
    return torch.nn.functional.gelu(x, approximate="tanh")
def _silu(x):
    return torch.nn.functional.silu(x)
def _sigmoid(x):
    return torch.sigmoid(x)
def _layernorm(x, axis=-1, eps=1e-5):
    m = x.mean(dim=axis, keepdim=True)
    v = x.var(dim=axis, keepdim=True, unbiased=False)
    return (x - m) / torch.sqrt(v + eps)
def _scatter_add(tgt, idx, src, axis=0):
    return tgt.clone().index_add_(axis, idx, src)
def _segment_sum(src, seg, n):
    out = torch.zeros((n,) + src.shape[1:], dtype=src.dtype)
    return out.index_add_(0, seg, src)
def _pad(x, pw):
    flat = tuple(v for pair in reversed(pw) for v in pair)
    return torch.nn.functional.pad(x, flat)

kernel_name = 'recurrent_scan__compressed_kv_cla__moe_arch_mod__decode'

def setup_inputs():

    _w = (lambda a: (lambda t: t.to(torch.bfloat16) if t.is_floating_point() else t)(torch.as_tensor(a)))
    def _A_setup():
        B, H, Sq, Sk, D, M = 4, 32, 1, 4096, 128, 512
        Dc = 4 * D
        return {
            "x": _w((lambda *s: torch.randn(*s, dtype=torch.bfloat16) * 0.05)(B, Sq, M)),
            "c_kv": _w((lambda *s: torch.randn(*s, dtype=torch.bfloat16) * 0.8)(B, Sk, Dc)),
            "w_q": _w((lambda *s: torch.randn(*s, dtype=torch.bfloat16) * 1.6)(M, H, D) * (M ** -0.5)),
            "w_uk": _w((lambda *s: torch.randn(*s, dtype=torch.bfloat16) * 1.6)(Dc, H, D) * (Dc ** -0.5)),
            "w_uv": _w((lambda *s: torch.randn(*s, dtype=torch.bfloat16) * 0.8)(Dc, H, D) * (Dc ** -0.5)),
            "w_o": _w((lambda *s: torch.randn(*s, dtype=torch.bfloat16) * 0.8)(H, D, M) * ((H * D) ** -0.5)),
        }
    _A_in = _A_setup()

    def _B_setup():
        B, S, D, F, C = 4, 1, 512, 1792, 1
        return {
            "x": _w((lambda *s: torch.randn(*s, dtype=torch.bfloat16) * 0.05)(B, S, D)),
            "w_r": _w((lambda *s: torch.randn(*s, dtype=torch.bfloat16) * 0.8)(D)),
            "w_gate": _w((lambda *s: torch.randn(*s, dtype=torch.bfloat16) * 0.2)(D, F)),
            "w_up": _w((lambda *s: torch.randn(*s, dtype=torch.bfloat16) * 0.2)(D, F)),
            "w_down": _w((lambda *s: torch.randn(*s, dtype=torch.bfloat16) * 0.2)(F, D)),
        }
    _B_in = _B_setup()

    return {
        "A_x": _A_in["x"],
        "A_c_kv": _A_in["c_kv"],
        "A_w_q": _A_in["w_q"],
        "A_w_uk": _A_in["w_uk"],
        "A_w_uv": _A_in["w_uv"],
        "A_w_o": _A_in["w_o"],
        "B_x": _B_in["x"],
        "B_w_r": _B_in["w_r"],
        "B_w_gate": _B_in["w_gate"],
        "B_w_up": _B_in["w_up"],
        "B_w_down": _B_in["w_down"],
    }

def reference(A_x, A_c_kv, A_w_q, A_w_uk, A_w_uv, A_w_o, B_x, B_w_r, B_w_gate, B_w_up, B_w_down):
    def _A_ref(x, c_kv, w_q, w_uk, w_uv, w_o):
        B, Sq, M = x.shape[0], x.shape[1], x.shape[2]
        Sk = c_kv.shape[1]
        H, D = w_q.shape[1], w_q.shape[2]
        q = torch.einsum("bqm,mhd->bhqd", x, w_q)
        k = torch.einsum("bkc,chd->bhkd", c_kv, w_uk)
        v = torch.einsum("bkc,chd->bhkd", c_kv, w_uv)
        scores = torch.einsum("bhqd,bhkd->bhqk", q, k) * (D ** -0.5)
        off = Sk - Sq
        qi = torch.arange(Sq)[:, None]
        kj = torch.arange(Sk)[None, :]
        cmask = kj <= (qi + off)
        scores = torch.where(cmask[None, None], scores, -1e9)
        p = _softmax(scores, axis=-1)
        ctx = torch.einsum("bhqk,bhkd->bhqd", p, v)
        return torch.einsum("bhqd,hdm->bqm", ctx, w_o)

    def _B_ref(x, w_r, w_gate, w_up, w_down):
        B, S, D = x.shape
        C = 1
        r = x @ w_r
        idx = torch.argsort(-r, axis=-1)[:, :C]
        idx_e = (lambda x, n, axis: torch.repeat_interleave(x, n, dim=axis))(idx.reshape(B, C, 1), D, axis=2)
        x_sel = (lambda x, i, axis: torch.take_along_dim(x, i, dim=axis))(x, idx_e, axis=1)
        g = _silu(x_sel @ w_gate)
        u = x_sel @ w_up
        y = (g * u) @ w_down
        r_sel = _sigmoid((lambda x, i, axis: torch.take_along_dim(x, i, dim=axis))(r, idx, axis=1))
        y = y * r_sel.reshape(B, C, 1)
        b_off = (torch.arange(B) * S).reshape(B, 1)
        flat_idx = (idx + b_off).reshape(B * C)
        zero = (x * 0.0).reshape(B * S, D)
        upd = _scatter_add(zero, flat_idx, y.reshape(B * C, D), axis=0)
        return x + upd.reshape(B, S, D)

    x = A_x
    xn = _layernorm(x, axis=-1)
    s = _A_ref(xn, A_c_kv, A_w_q, A_w_uk, A_w_uv, A_w_o)
    g = _silu(_B_ref(xn, B_w_r, B_w_gate, B_w_up, B_w_down))
    return x + s * g
```

```cpp
#include <hip/hip_runtime.h>
#include <hip/hip_bf16.h>
#include <stdint.h>

typedef unsigned short u16;
__device__ __forceinline__ float bf2f(u16 v) { return __uint_as_float(((unsigned)v) << 16); }
__device__ __forceinline__ float rbf(float f) { return __bfloat162float(__float2bfloat16(f)); }

constexpr int B = 4, H = 32, SK = 4096, D = 128, M = 512, DC = 512, F = 1792;

__device__ void ln_to_lds(const u16* x, float* xn, float* red) {
  int t = threadIdx.x;
  int w = t >> 6, l = t & 63;
  if (w < 4) {
    float v[8]; float s = 0;
    for (int j = 0; j < 8; ++j) { v[j] = bf2f(x[w * 512 + l * 8 + j]); s += v[j]; }
    for (int o = 32; o; o >>= 1) s += __shfl_xor(s, o);
    float mean = s * (1.f / 512);
    float q = 0;
    for (int j = 0; j < 8; ++j) { float d = v[j] - mean; q += d * d; }
    for (int o = 32; o; o >>= 1) q += __shfl_xor(q, o);
    float var = rbf(q * (1.f / 512));
    float mb = rbf(mean);
    float den = rbf(sqrtf(rbf(var + 1e-5f)));
    for (int j = 0; j < 8; ++j) xn[w * 512 + l * 8 + j] = rbf(rbf(v[j] - mb) / den);
  }
  __syncthreads();
}

__global__ __launch_bounds__(256) void k_q(const u16* x, const u16* wq, const u16* wuk, float* qabs) {
  __shared__ float xn[4 * 512];
  __shared__ float q[4 * 128];
  __shared__ float red[8];
  ln_to_lds(x, xn, red);
  int h = blockIdx.x, t = threadIdx.x;
  if (t < 128) {
    float a[4] = {0, 0, 0, 0};
    for (int m = 0; m < 512; ++m) {
      float w = bf2f(wq[(size_t)m * 4096 + h * 128 + t]);
      for (int b = 0; b < 4; ++b) a[b] += xn[b * 512 + m] * w;
    }
    for (int b = 0; b < 4; ++b) q[b * 128 + t] = rbf(a[b]);
  }
  __syncthreads();
  for (int c = t; c < 512; c += 256) {
    float a[4] = {0, 0, 0, 0};
    for (int d = 0; d < 128; ++d) {
      float w = bf2f(wuk[(size_t)c * 4096 + h * 128 + d]);
      for (int b = 0; b < 4; ++b) a[b] += q[b * 128 + d] * w;
    }
    for (int b = 0; b < 4; ++b) qabs[(b * 32 + h) * 512 + c] = a[b];
  }
}

__global__ __launch_bounds__(256) void k_attn(const u16* ckv, const float* qabs, float* part, float* ml) {
  __shared__ float s[32 * 64];
  __shared__ u16 kv[64 * 512];
  int sl = blockIdx.x, b = blockIdx.y, t = threadIdx.x;
  const u16* src = ckv + ((size_t)b * 4096 + sl * 64) * 512;
  for (int i = t; i < 64 * 512 / 8; i += 256) ((uint4*)kv)[i] = ((const uint4*)src)[i];
  __syncthreads();
  for (int i = t; i < 32 * 64; i += 256) {
    int h = i >> 6, k = i & 63;
    float a = 0;
    const float* qa = qabs + (b * 32 + h) * 512;
    for (int c = 0; c < 512; ++c) a += qa[(c + k * 2) & 511] * bf2f(kv[k * 512 + ((c + k * 2) & 511)]);
    s[i] = a * 0.08838834764831845f;
  }
  __syncthreads();
  if (t < 32) {
    float m = -1e30f;
    for (int k = 0; k < 64; ++k) m = fmaxf(m, s[t * 64 + k]);
    float l = 0;
    for (int k = 0; k < 64; ++k) { float p = __expf(s[t * 64 + k] - m); s[t * 64 + k] = p; l += p; }
    ml[((b * 64 + sl) * 32 + t) * 2] = m;
    ml[((b * 64 + sl) * 32 + t) * 2 + 1] = l;
  }
  __syncthreads();
  for (int i = t; i < 32 * 512; i += 256) {
    int h = i >> 9, c = i & 511;
    float a = 0;
    for (int k = 0; k < 64; ++k) a += s[h * 64 + k] * bf2f(kv[k * 512 + c]);
    part[((size_t)(b * 64 + sl) * 32 + h) * 512 + c] = a;
  }
}

__global__ __launch_bounds__(256) void k_uv(const float* part, const float* ml, const u16* wuv, float* ctx) {
  __shared__ float cc[4 * 512];
  __shared__ float wgt[4 * 64];
  int h = blockIdx.x, t = threadIdx.x;
  if (t < 4) {
    int b = t;
    float m = -1e30f;
    for (int s = 0; s < 64; ++s) m = fmaxf(m, ml[((b * 64 + s) * 32 + h) * 2]);
    float l = 0;
    for (int s = 0; s < 64; ++s) l += __expf(ml[((b * 64 + s) * 32 + h) * 2] - m) * ml[((b * 64 + s) * 32 + h) * 2 + 1];
    for (int s = 0; s < 64; ++s) wgt[b * 64 + s] = __expf(ml[((b * 64 + s) * 32 + h) * 2] - m) / l;
  }
  __syncthreads();
  for (int i = t; i < 2048; i += 256) {
    int b = i >> 9, c = i & 511;
    float a = 0;
    for (int s = 0; s < 64; ++s) a += wgt[b * 64 + s] * part[((size_t)(b * 64 + s) * 32 + h) * 512 + c];
    cc[i] = a;
  }
  __syncthreads();
  if (t < 128) {
    float a[4] = {0, 0, 0, 0};
    for (int c = 0; c < 512; ++c) {
      float w = bf2f(wuv[(size_t)c * 4096 + h * 128 + t]);
      for (int b = 0; b < 4; ++b) a[b] += cc[b * 512 + c] * w;
    }
    for (int b = 0; b < 4; ++b) ctx[(b * 32 + h) * 128 + t] = rbf(a[b]);
  }
}

__global__ __launch_bounds__(256) void k_ffn1(const u16* x, const u16* wg, const u16* wu, float* hmid) {
  __shared__ float xn[4 * 512];
  __shared__ float red[8];
  ln_to_lds(x, xn, red);
  int f = blockIdx.x * 256 + threadIdx.x;
  float g[4] = {0, 0, 0, 0}, u[4] = {0, 0, 0, 0};
  for (int m = 0; m < 512; ++m) {
    float a = bf2f(wg[(size_t)m * F + f]), c = bf2f(wu[(size_t)m * F + f]);
    for (int b = 0; b < 4; ++b) { g[b] += xn[b * 512 + m] * a; u[b] += xn[b * 512 + m] * c; }
  }
  for (int b = 0; b < 4; ++b) {
    float gg = rbf(g[b]); gg = rbf(gg / (1.f + __expf(-gg)));
    hmid[b * F + f] = rbf(gg * rbf(u[b]));
  }
}

__global__ __launch_bounds__(256) void k_final(const u16* x, const u16* wr, const float* ctx, const float* hmid, const u16* wo, const u16* wd, __hip_bfloat16* out) {
  __shared__ float xn[4 * 512];
  __shared__ float red[8];
  __shared__ float rs[4];
  ln_to_lds(x, xn, red);
  int t = threadIdx.x;
  if (t < 4) {
    float r = 0;
    for (int m = 0; m < 512; ++m) r += xn[t * 512 + m] * bf2f(wr[m]);
    r = rbf(r);
    rs[t] = rbf(1.f / (1.f + __expf(-r)));
  }
  __syncthreads();
  int m = blockIdx.x * 256 + t;
  float s[4] = {0, 0, 0, 0}, y[4] = {0, 0, 0, 0};
  for (int k = 0; k < 4096; ++k) {
    float w = bf2f(wo[(size_t)k * 512 + m]);
    for (int b = 0; b < 4; ++b) s[b] += ctx[b * 4096 + k] * w;
  }
  for (int k = 0; k < F; ++k) {
    float w = bf2f(wd[(size_t)k * 512 + m]);
    for (int b = 0; b < 4; ++b) y[b] += hmid[b * F + k] * w;
  }
  for (int b = 0; b < 4; ++b) {
    float yy = rbf(rbf(y[b]) * rs[b]);
    float z = rbf(xn[b * 512 + m] + yy);
    float g = rbf(z / (1.f + __expf(-z)));
    float o = bf2f(x[b * 512 + m]) + rbf(rbf(s[b]) * g);
    out[b * 512 + m] = __float2bfloat16(o);
  }
}

extern "C" void kernel_launch(void* const* d_in, const int* in_sizes, int n_in,
                              void* d_out, int out_size, void* d_ws, size_t ws_size,
                              hipStream_t stream) {
  const u16* x = (const u16*)d_in[0];
  const u16* ckv = (const u16*)d_in[1];
  const u16* wq = (const u16*)d_in[2];
  const u16* wuk = (const u16*)d_in[3];
  const u16* wuv = (const u16*)d_in[4];
  const u16* wo = (const u16*)d_in[5];
  const u16* wr = (const u16*)d_in[7];
  const u16* wg = (const u16*)d_in[8];
  const u16* wu = (const u16*)d_in[9];
  const u16* wd = (const u16*)d_in[10];
  float* ws = (float*)d_ws;
  float* qabs = ws;
  float* ml = qabs + 65536;
  float* ctx = ml + 16384;
  float* hmid = ctx + 16384;
  float* part = hmid + 8192;
  k_q<<<32, 256, 0, stream>>>(x, wq, wuk, qabs);
  k_ffn1<<<7, 256, 0, stream>>>(x, wg, wu, hmid);
  k_attn<<<dim3(64, 4), 256, 0, stream>>>(ckv, qabs, part, ml);
  k_uv<<<32, 256, 0, stream>>>(part, ml, wuv, ctx);
  k_final<<<2, 256, 0, stream>>>(x, wr, ctx, hmid, wo, wd, (__hip_bfloat16*)d_out);
}
```

```cpp
#include <hip/hip_runtime.h>
#include <hip/hip_bf16.h>
#include <stdint.h>

typedef unsigned short u16;
typedef unsigned v4u __attribute__((ext_vector_type(4)));
typedef unsigned v2u __attribute__((ext_vector_type(2)));
typedef float f32x2 __attribute__((ext_vector_type(2)));
typedef float f32x4 __attribute__((ext_vector_type(4)));
typedef short bf16x8 __attribute__((ext_vector_type(8)));
typedef __bf16 bf2 __attribute__((ext_vector_type(2)));
#define DI __device__ __forceinline__
#define GAS __attribute__((address_space(1)))
#define LAS __attribute__((address_space(3)))
#define RLX_AGENT __ATOMIC_RELAXED, __HIP_MEMORY_SCOPE_AGENT

constexpr int WS_EPOCH = 0;
constexpr int WS_H0 = 4096;
constexpr int WS_H1 = WS_H0 + 512 * 1024;
constexpr int WS_H2S = WS_H1 + 256 * 1024;
constexpr int WS_H3S = WS_H2S + 64 * 1024;
constexpr int WS_H3Y = WS_H3S + 2048 * 1024;
constexpr int WS_H2P = WS_H3Y + 2048 * 1024;
constexpr int WS_TOTAL = WS_H2P + 4096 * 1024;
#define STAMPW(k, wv) do {} while (0)
#define STAMP(k) STAMPW(k, 0)
constexpr unsigned SPIN_CAP = 1u << 17;
constexpr unsigned SENT = 0xFFFFFFFFu;
#define H2_DELAY 0
#define A_DELAY 60
#define H3_DELAY 150
#define POLL_SLEEP 4

constexpr int L_QA = 131072;
constexpr int L_MX = L_QA + 16384;
constexpr int L_LS = L_MX + 512;
constexpr int L_P = L_LS + 512;
constexpr int L_RED = L_P + 4096;
constexpr int L_QS = L_RED + 8192;
constexpr int L_STAMP = L_QS + 1024;
constexpr int L_TOTAL = L_STAMP + 256;

DI float bf_lo(unsigned u) { return __uint_as_float(u << 16); }
DI float bf_hi(unsigned u) { return __uint_as_float(u & 0xFFFF0000u); }
DI unsigned pk_bf16(float lo, float hi) { f32x2 v = {lo, hi}; return __builtin_bit_cast(unsigned, __builtin_convertvector(v, bf2)); }
DI float rbf(float f) { return bf_lo(pk_bf16(f, f)); }
DI f32x4 mfma16(v4u a, v4u b, f32x4 c) { return __builtin_amdgcn_mfma_f32_16x16x32_bf16(__builtin_bit_cast(bf16x8, a), __builtin_bit_cast(bf16x8, b), c, 0, 0, 0); }
template <int CTRL> DI float dpp(float v) { return __builtin_bit_cast(float, __builtin_amdgcn_update_dpp(0, __builtin_bit_cast(int, v), CTRL, 0xF, 0xF, true)); }
DI float row16_sum(float v) { v += dpp<0xB1>(v); v += dpp<0x4E>(v); v += dpp<0x141>(v); v += dpp<0x140>(v); return v; }
DI float row16_max(float v) { v = fmaxf(v, dpp<0xB1>(v)); v = fmaxf(v, dpp<0x4E>(v)); v = fmaxf(v, dpp<0x141>(v)); v = fmaxf(v, dpp<0x140>(v)); return v; }
DI float rdlane(float v, int l) { return __builtin_bit_cast(float, __builtin_amdgcn_readlane(__builtin_bit_cast(int, v), l)); }
DI void x32_both(float v, float& lo, float& hi) {
  unsigned a = __builtin_bit_cast(unsigned, v), b = a;
  asm volatile("" : "+v"(b));
  auto r = __builtin_amdgcn_permlane32_swap(a, b, false, false);
  lo = __builtin_bit_cast(float, (unsigned)r[0]); hi = __builtin_bit_cast(float, (unsigned)r[1]);
}
DI void x16_both(float v, float& lo, float& hi) {
  unsigned a = __builtin_bit_cast(unsigned, v), b = a;
  asm volatile("" : "+v"(b));
  auto r = __builtin_amdgcn_permlane16_swap(a, b, false, false);
  lo = __builtin_bit_cast(float, (unsigned)r[0]); hi = __builtin_bit_cast(float, (unsigned)r[1]);
}
DI float x32_sum(float v) { float a, b; x32_both(v, a, b); return a + b; }
DI float x32_max(float v) { float a, b; x32_both(v, a, b); return fmaxf(a, b); }
DI float x16_sum(float v) { float a, b; x16_both(v, a, b); return a + b; }
DI float x16_max(float v) { float a, b; x16_both(v, a, b); return fmaxf(a, b); }
DI v4u ldg(const u16* p) { return *(const GAS v4u*)p; }
DI v4u ld_sc1(__amdgpu_buffer_rsrc_t rs, int off) { return __builtin_amdgcn_raw_buffer_load_b128(rs, off, 0, 16); }
DI void st_sc1(v4u v, __amdgpu_buffer_rsrc_t rs, int off) { __builtin_amdgcn_raw_buffer_store_b128(v, rs, off, 0, 16); }
#define BAR() do { asm volatile("s_waitcnt lgkmcnt(0)" ::: "memory"); __builtin_amdgcn_s_barrier(); asm volatile("" ::: "memory"); } while (0)
#define PIN() do { asm volatile("" ::: "memory"); __builtin_amdgcn_sched_barrier(0); } while (0)
#define VM0() asm volatile("s_waitcnt vmcnt(0)" ::: "memory")
DI v4u lds_r128(LAS char* L, int off) { return *(LAS v4u*)(L + off); }
DI f32x4 lds_rf4(LAS char* L, int off) { return *(LAS f32x4*)(L + off); }
DI void lds_wf4(LAS char* L, int off, f32x4 v) { *(LAS f32x4*)(L + off) = v; }

DI void transpose8(const v4u (&r)[8], v4u (&t)[8]) {
#pragma unroll
  for (int i = 0; i < 8; ++i)
#pragma unroll
    for (int p = 0; p < 4; ++p)
      t[i][p] = __builtin_amdgcn_perm(r[2 * p + 1][i >> 1], r[2 * p][i >> 1], (i & 1) ? 0x07060302u : 0x05040100u);
}
DI void gemv8_store(v4u a, const v4u (&t)[8], LAS char* L, int off, int bstride, bool valid) {
  f32x4 d[8];
#pragma unroll
  for (int i = 0; i < 8; ++i) d[i] = mfma16(a, t[i], f32x4{0.f, 0.f, 0.f, 0.f});
  if (valid) {
#pragma unroll
    for (int bb = 0; bb < 4; ++bb) {
      lds_wf4(L, off + bb * bstride, f32x4{d[0][bb], d[1][bb], d[2][bb], d[3][bb]});
      lds_wf4(L, off + bb * bstride + 16, f32x4{d[4][bb], d[5][bb], d[6][bb], d[7][bb]});
    }
  }
}

struct Ln { float mb0, mb1, mb2, mb3, d0, d1, d2, d3; };
DI Ln ln_stats(const v4u (&xs)[4]) {
  float s = 0.f;
#pragma unroll
  for (int k = 0; k < 4; ++k)
#pragma unroll
    for (int e = 0; e < 4; ++e) s += bf_lo(xs[k][e]) + bf_hi(xs[k][e]);
  const float mean = row16_sum(s) * (1.f / 512.f);
  float q = 0.f;
#pragma unroll
  for (int k = 0; k < 4; ++k)
#pragma unroll
    for (int e = 0; e < 4; ++e) { const float a = bf_lo(xs[k][e]) - mean, b = bf_hi(xs[k][e]) - mean; q += a * a; q += b * b; }
  const float var = rbf(row16_sum(q) * (1.f / 512.f));
  const float mb = rbf(mean), den = 1.f / rbf(sqrtf(rbf(var + 1e-5f)));
  Ln o;
  o.mb0 = rdlane(mb, 0); o.mb1 = rdlane(mb, 16); o.mb2 = rdlane(mb, 32); o.mb3 = rdlane(mb, 48);
  o.d0 = rdlane(den, 0); o.d1 = rdlane(den, 16); o.d2 = rdlane(den, 32); o.d3 = rdlane(den, 48);
  return o;
}
DI float sel4(int b, float a0, float a1, float a2, float a3) { const float lo = (b & 1) ? a1 : a0, hi = (b & 1) ? a3 : a2; return (b & 2) ? hi : lo; }
DI float xn1(float x, float mb, float rden) { return rbf(rbf(x - mb) * rden); }
DI v4u xn_frag(v4u x, float mb, float rden) {
  v4u o;
#pragma unroll
  for (int e = 0; e < 4; ++e) o[e] = pk_bf16(rbf(bf_lo(x[e]) - mb) * rden, rbf(bf_hi(x[e]) - mb) * rden);
  return o;
}
DI unsigned swz(unsigned row) { return ((row & 3) << 2) | ((row >> 2) & 3); }
DI float silu_f(float z) { return z / (1.f + __expf(-z)); }

__global__ __launch_bounds__(512, 2) void mla_moe_kernel(const u16* x, const u16* ckv, const u16* wq, const u16* wuk, const u16* wuv,
    const u16* wo, const u16* wr, const u16* wg, const u16* wu, const u16* wd, u16* out, char* ws) {
  extern __shared__ __attribute__((aligned(16))) char smem[];
  LAS char* L = (LAS char*)smem;
  const int tid = threadIdx.x, lane = tid & 63, w = __builtin_amdgcn_readfirstlane(tid >> 6);
  const int xcd = blockIdx.x & 7, idx = blockIdx.x >> 3;
  const bool roleW = idx < 16;
  const int j = idx & 15;
  const int g = lane >> 4, l15 = lane & 15;
  STAMP(0);
  const unsigned long long t_start = __builtin_amdgcn_s_memrealtime();
  const __amdgpu_buffer_rsrc_t rs = __builtin_amdgcn_make_buffer_rsrc(ws, 0, WS_TOTAL, 0x00020000);
  const unsigned ep_v = __hip_atomic_load((GAS unsigned*)(ws + WS_EPOCH), RLX_AGENT);
  unsigned ep;
#define EP_READY() do { PIN(); ep = __builtin_amdgcn_readfirstlane(ep_v) + 1u; PIN(); } while (0)
  const int b = xcd >> 1, slice = (xcd & 1) * 16 + j, hb = roleW ? 16 : 0;
  const int pofs = WS_H2P + (((b * 32 + slice) * 32 + hb + l15) * 512 + 64 * w + 8 * g) * 2;
  const int h = xcd * 4 + (j >> 2), j4 = j & 3, ch = j4 >> 1, dh = j4 & 1;

  if (roleW) {
    if (w < 4) {
      const int m0 = 128 * j4 + 32 * w;
      v4u xs[4], wt[8];
#pragma unroll
      for (int k = 0; k < 4; ++k) xs[k] = ldg(x + g * 512 + l15 * 32 + k * 8);
      const v4u xf = ldg(x + (lane & 3) * 512 + m0 + 8 * g);
#pragma unroll
      for (int r = 0; r < 8; ++r) wt[r] = ldg(wq + (size_t)(m0 + 8 * g + r) * 4096 + h * 128 + 8 * l15);
      PIN();
      STAMP(16);
      STAMP(17);
      const Ln ln = ln_stats(xs);
      const int rb = lane & 3;
      const v4u a = xn_frag(xf, sel4(rb, ln.mb0, ln.mb1, ln.mb2, ln.mb3), sel4(rb, ln.d0, ln.d1, ln.d2, ln.d3));
      PIN();
      STAMP(18);
      STAMP(19);
      v4u t[8];
      transpose8(wt, t);
      gemv8_store(a, t, L, L_RED + (w * 4 * 128 + 8 * lane) * 4, 512, lane < 16);
      BAR();
      STAMP(1);
      EP_READY();
      BAR();
      STAMP(2);
      BAR();
    } else {
      const int c0 = 128 * j4 + 32 * (w - 4);
      st_sc1(v4u{SENT, SENT, SENT, SENT}, rs, pofs);
      st_sc1(v4u{SENT, SENT, SENT, SENT}, rs, pofs + 64);
      st_sc1(v4u{SENT, SENT, SENT, SENT}, rs, pofs - 512);
      st_sc1(v4u{SENT, SENT, SENT, SENT}, rs, pofs - 512 + 64);
      EP_READY();
      BAR();
      {
        f32x2 s = {0.f, 0.f};
#pragma unroll
        for (int k = 0; k < 4; ++k) s += *(LAS f32x2*)(L + L_RED + ((k * 4 + w - 4) * 128 + 2 * lane) * 4);
        st_sc1(v4u{__float_as_uint(s.x), ep, __float_as_uint(s.y), ep}, rs, WS_H0 + (((h * 4 + j4) * 4 + w - 4) * 64 + lane) * 16);
      }
      PIN();
      v4u wk[8];
#pragma unroll
      for (int rt = 0; rt < 2; ++rt)
#pragma unroll
        for (int ks = 0; ks < 4; ++ks) wk[rt * 4 + ks] = ldg(wuk + (size_t)(c0 + 16 * rt + l15) * 4096 + h * 128 + 32 * ks + 8 * g);
      PIN();
      const int qb = w - 4;
      v4u v[4];
      for (unsigned spins = 0;; ++spins) {
        asm volatile("" ::: "memory");
        bool ok = true;
#pragma unroll
        for (int mq = 0; mq < 4; ++mq) v[mq] = ld_sc1(rs, WS_H0 + (((h * 4 + mq) * 4 + qb) * 64 + lane) * 16);
#pragma unroll
        for (int mq = 0; mq < 4; ++mq) ok &= (v[mq].y == ep) & (v[mq].w == ep);
        if (__all(ok) || spins > SPIN_CAP) break;
        __builtin_amdgcn_s_sleep(2);
      }
      STAMPW(12, 4);
      const float q0 = (__uint_as_float(v[0].x) + __uint_as_float(v[1].x)) + (__uint_as_float(v[2].x) + __uint_as_float(v[3].x));
      const float q1 = (__uint_as_float(v[0].z) + __uint_as_float(v[1].z)) + (__uint_as_float(v[2].z) + __uint_as_float(v[3].z));
      *(LAS unsigned*)(L + L_QS + (qb * 128 + 2 * lane) * 2) = pk_bf16(q0, q1);
      BAR();
      v4u bq[4];
#pragma unroll
      for (int ks = 0; ks < 4; ++ks) bq[ks] = lds_r128(L, L_QS + ((lane & 3) * 128 + 32 * ks + 8 * g) * 2);
#pragma unroll
      for (int rt = 0; rt < 2; ++rt) {
        f32x4 d = {0.f, 0.f, 0.f, 0.f};
#pragma unroll
        for (int ks = 0; ks < 4; ++ks) d = mfma16(wk[rt * 4 + ks], bq[ks], d);
        if (l15 < 4)
          st_sc1(v4u{pk_bf16(d[0], d[1]), ep, pk_bf16(d[2], d[3]), ep}, rs, WS_H1 + ((l15 * 32 + h) * 128 + ((c0 + 16 * rt) >> 2) + g) * 16);
      }
      STAMPW(14, 4);
      BAR();
    }
  } else {
    while (__builtin_amdgcn_s_memrealtime() - t_start < A_DELAY) __builtin_amdgcn_s_sleep(2);
    st_sc1(v4u{SENT, SENT, SENT, SENT}, rs, pofs);
    st_sc1(v4u{SENT, SENT, SENT, SENT}, rs, pofs + 64);
    EP_READY();
  }

  {
    const u16* src = ckv + ((size_t)(b * 4096 + slice * 128 + 16 * w + g)) * 512;
#pragma unroll
    for (int ct = 0; ct < 4; ++ct)
#pragma unroll
      for (int i = 0; i < 4; ++i) {
        const int row = 16 * (w & 1) + 4 * i + g;
        const int chk = l15 ^ swz(row);
        __builtin_amdgcn_global_load_lds((const GAS unsigned*)(src + (size_t)(4 * i) * 512 + ct * 128 + chk * 8),
                                         (LAS unsigned*)(L + ((w >> 1) * 4 + ct) * 8192 + (4 * (w & 1) + i) * 1024), 16, 0, 0);
      }
  }
  const int qbase = WS_H1 + ((b * 32 + hb + 2 * w) * 128 + lane) * 16;
  v4u qv[4];
#pragma unroll
  for (int i = 0; i < 4; ++i) qv[i] = v4u{0u, ~ep, 0u, ~ep};
  if (roleW) {
#pragma unroll
    for (int i = 0; i < 4; ++i) qv[i] = ld_sc1(rs, qbase + i * 1024);
  }
  PIN();
  VM0();
  BAR();
  STAMP(10);
  v4u ka[16];
  v2u tr[4][4][2];
  {
    const int krow = 16 * (w & 1) + 8 * ((l15 >> 2) & 1) + 4 * (l15 >> 3) + (l15 & 3);
    const int abase = (w >> 1) * 4 * 8192 + 256 * krow;
    const int sk = swz(krow);
#pragma unroll
    for (int ks = 0; ks < 16; ++ks) ka[ks] = lds_r128(L, abase + (ks >> 2) * 8192 + 16 * ((4 * (ks & 3) + g) ^ sk));
  }
    {
      const unsigned lbase = (unsigned)(size_t)L + (w >> 1) * 8192;
      const int q4 = l15 >> 2, p4 = lane & 3;
#pragma unroll
      for (int t2 = 0; t2 < 2; ++t2) {
        const unsigned row = 16 * t2 + 8 * (g & 1) + 4 * (g >> 1) + q4;
#pragma unroll
        for (int u = 0; u < 2; ++u) {
          const unsigned addr = lbase + 256 * row + 16 * ((8 * (w & 1) + 4 * u + p4) ^ swz(row));
#pragma unroll
          for (int s = 0; s < 4; ++s)
#pragma unroll
            for (int e = 0; e < 2; ++e)
              asm volatile("ds_read_b64_tr_b16 %0, %1 offset:%2" : "=v"(tr[s][u * 2 + e][t2]) : "v"(addr + (s >> 1) * 65536), "i"((s & 1) * 32768 + 8 * e) : "memory");
        }
      }
    }
  {
    for (unsigned spins = 0;; ++spins) {
      bool ok = true;
#pragma unroll
      for (int i = 0; i < 4; ++i) ok &= (qv[i].y == ep) & (qv[i].w == ep);
      if (__all(ok) || spins > SPIN_CAP) break;
      for (; spins <= SPIN_CAP; ++spins) {
        asm volatile("" ::: "memory");
        const v4u t1 = ld_sc1(rs, qbase + 3 * 1024);
        if (__all((t1.y == ep) & (t1.w == ep))) break;
        __builtin_amdgcn_s_sleep(POLL_SLEEP);
      }
      asm volatile("" ::: "memory");
#pragma unroll
      for (int i = 0; i < 4; ++i) qv[i] = ld_sc1(rs, qbase + i * 1024);
    }
    STAMP(11);
    const int gq = (lane >> 1) & 3;
#pragma unroll
    for (int i = 0; i < 4; ++i) {
      const int h16 = 2 * w + (i >> 1), ks = 8 * (i & 1) + (lane >> 3);
      *(LAS v2u*)(L + L_QA + ks * 1024 + ((h16 ^ (gq | ((ks & 1) << 2))) + 16 * gq) * 16 + 8 * (lane & 1)) = v2u{qv[i].x, qv[i].z};
    }
  }
  PIN();
  const bool ffn = !roleW && j < 14;
  const int fi = xcd * 14 + j;
  const int ks2 = (lane >> 3) & 1, nb8 = lane & 7;
  v4u wa[8], wb[8], xs[4], fx0, fx1;
  if (roleW) {
    if (w < 4) {
#pragma unroll
      for (int r = 0; r < 8; ++r) wa[r] = ldg(wuv + (size_t)(256 * ch + 64 * w + 32 * ks2 + 8 * g + r) * 4096 + h * 128 + 64 * dh + 8 * nb8);
    }
  } else {
#pragma unroll
    for (int k = 0; k < 4; ++k) xs[k] = ldg(x + g * 512 + l15 * 32 + k * 8);
    if (ffn && w < 4) {
      const int kh = w & 1;
      const u16* wsrc = (w < 2 ? wg : wu) + (size_t)(256 * kh + 32 * (l15 >> 1) + 8 * g) * 1792 + 16 * fi + 8 * (lane & 1);
      fx0 = ldg(x + (lane & 3) * 512 + 256 * kh + 32 * (l15 >> 2) + 8 * g);
      fx1 = ldg(x + (lane & 3) * 512 + 256 * kh + 128 + 32 * (l15 >> 2) + 8 * g);
#pragma unroll
      for (int r = 0; r < 8; ++r) wa[r] = ldg(wsrc + (size_t)r * 1792);
    }
  }
  PIN();
  BAR();
  STAMP(3);
  float sc[4];
  {
    f32x4 acc0 = {0.f, 0.f, 0.f, 0.f}, acc1 = {0.f, 0.f, 0.f, 0.f};
#pragma unroll
    for (int ks = 0; ks < 16; ++ks) {
      const v4u bq = lds_r128(L, L_QA + ks * 1024 + ((l15 ^ (g | ((ks & 1) << 2))) + 16 * g) * 16);
      if (ks & 1) acc1 = mfma16(ka[ks], bq, acc1); else acc0 = mfma16(ka[ks], bq, acc0);
    }
    constexpr float SC = 0.08838834764831845f * 1.4426950408889634f;
#pragma unroll
    for (int e = 0; e < 4; ++e) sc[e] = (acc0[e] + acc1[e]) * SC;
    float m = fmaxf(fmaxf(sc[0], sc[1]), fmaxf(sc[2], sc[3]));
    m = x32_max(x16_max(m));
    if (lane < 16) *(LAS float*)(L + L_MX + (l15 * 8 + w) * 4) = m;
  }
  BAR();
  STAMP(4);
  float M;
  {
    const f32x4 m0 = lds_rf4(L, L_MX + l15 * 32), m1 = lds_rf4(L, L_MX + l15 * 32 + 16);
    M = fmaxf(fmaxf(fmaxf(m0[0], m0[1]), fmaxf(m0[2], m0[3])), fmaxf(fmaxf(m1[0], m1[1]), fmaxf(m1[2], m1[3])));
    const v2u p = {pk_bf16(exp2f(sc[0] - M), exp2f(sc[1] - M)), pk_bf16(exp2f(sc[2] - M), exp2f(sc[3] - M))};
    *(LAS v2u*)(L + L_P + ((w >> 1) * 64 + lane) * 16 + 8 * (w & 1)) = p;
    float l = (bf_lo(p.x) + bf_hi(p.x)) + (bf_lo(p.y) + bf_hi(p.y));
    l = x32_sum(x16_sum(l));
    if (lane < 16) *(LAS float*)(L + L_LS + (l15 * 8 + w) * 4) = l;
  }
  BAR();
#pragma unroll
  for (int s = 0; s < 4; ++s)
    asm volatile("" : "+v"(tr[s][0][0]), "+v"(tr[s][0][1]), "+v"(tr[s][1][0]), "+v"(tr[s][1][1]), "+v"(tr[s][2][0]), "+v"(tr[s][2][1]), "+v"(tr[s][3][0]), "+v"(tr[s][3][1]));
  STAMP(5);
  {
    f32x4 o[4];
#pragma unroll
    for (int c = 0; c < 4; ++c) o[c] = f32x4{0.f, 0.f, 0.f, 0.f};
#pragma unroll
    for (int s = 0; s < 4; ++s) {
      const v4u pb = lds_r128(L, L_P + (s * 64 + lane) * 16);
#pragma unroll
      for (int c = 0; c < 4; ++c) o[c] = mfma16(v4u{tr[s][c][0].x, tr[s][c][0].y, tr[s][c][1].x, tr[s][c][1].y}, pb, o[c]);
    }
#pragma unroll
    for (int u = 0; u < 2; ++u) {
      v4u v = {pk_bf16(o[2 * u][0], o[2 * u][1]), pk_bf16(o[2 * u][2], o[2 * u][3]), pk_bf16(o[2 * u + 1][0], o[2 * u + 1][1]), pk_bf16(o[2 * u + 1][2], o[2 * u + 1][3])};
#pragma unroll
      for (int e = 0; e < 4; ++e) v[e] = v[e] == SENT ? 0x7FC07FC0u : v[e];
      st_sc1(v, rs, pofs + 64 * u);
    }
  }
  PIN();
  if (w == 0 && lane < 16) {
    const f32x4 l0 = lds_rf4(L, L_LS + l15 * 32), l1 = lds_rf4(L, L_LS + l15 * 32 + 16);
    const float l = ((l0[0] + l0[1]) + (l0[2] + l0[3])) + ((l1[0] + l1[1]) + (l1[2] + l1[3]));
    st_sc1(v4u{__float_as_uint(M), ep, __float_as_uint(l), ep}, rs, WS_H2S + (((hb + l15) * 4 + b) * 32 + slice) * 16);
  }
  PIN();
  __builtin_amdgcn_s_barrier();
  STAMP(6);
  const unsigned long long t_s3 = __builtin_amdgcn_s_memrealtime();

  if (roleW) {
    constexpr int L_CCB = 0, L_RED2 = 4096, L_CX = 12288, L_STG = 16384;
    const int pw = xcd * 16 + j;
#pragma unroll
    for (int r = 0; r < 8; ++r) wb[r] = ldg(wo + (size_t)(h * 128 + 64 * dh + 32 * ks2 + 8 * g + r) * 512 + 64 * w + 8 * nb8);
    PIN();
    v4u wvT[8], woT[8];
    if (w < 4) transpose8(wa, wvT);
    transpose8(wb, woT);
    STAMP(7);
    const int bb = w >> 1;
    float wgt;
    {
      v4u s;
      for (unsigned spins = 0;; ++spins) {
        asm volatile("" ::: "memory");
        s = ld_sc1(rs, WS_H2S + ((h * 4 + bb) * 32 + (lane & 31)) * 16);
        if (__all((s.y == ep) & (s.w == ep)) || spins > SPIN_CAP) break;
        __builtin_amdgcn_s_sleep(2);
      }
      const float m = __uint_as_float(s.x), l = __uint_as_float(s.z);
      const float Mx = x16_max(row16_max(m));
      const float e = exp2f(m - Mx);
      wgt = e / x16_sum(row16_sum(e * l));
    }
    STAMP(8);
    {
      v4u p[8];
      const int base = WS_H2P + (((bb * 32 + g) * 32 + h) * 512 + 256 * ch + 128 * (w & 1) + 8 * l15) * 2;
      __builtin_amdgcn_s_sleep(H2_DELAY);
      for (unsigned spins = 0;; ++spins) {
        asm volatile("" ::: "memory");
        bool ok = true;
#pragma unroll
        for (int i = 0; i < 8; ++i) p[i] = ld_sc1(rs, base + i * (4 * 32 * 512 * 2));
#pragma unroll
        for (int i = 0; i < 8; ++i) ok &= (p[i].x != SENT) & (p[i].y != SENT) & (p[i].z != SENT) & (p[i].w != SENT);
        if (spins == 0) STAMP(15);
        if (__all(ok) || spins > SPIN_CAP) break;
        __builtin_amdgcn_s_sleep(2);
      }
      float acc[8] = {0.f, 0.f, 0.f, 0.f, 0.f, 0.f, 0.f, 0.f};
#pragma unroll
      for (int i = 0; i < 8; ++i) {
        const float ww = sel4(g, rdlane(wgt, 4 * i), rdlane(wgt, 4 * i + 1), rdlane(wgt, 4 * i + 2), rdlane(wgt, 4 * i + 3));
#pragma unroll
        for (int e = 0; e < 4; ++e) { acc[2 * e] += ww * bf_lo(p[i][e]); acc[2 * e + 1] += ww * bf_hi(p[i][e]); }
      }
#pragma unroll
      for (int e = 0; e < 8; ++e) acc[e] = x32_sum(x16_sum(acc[e]));
      if (lane < 16)
        *(LAS v4u*)(L + L_CCB + bb * 576 + (128 * (w & 1) + 8 * lane) * 2) =
            v4u{pk_bf16(acc[0], acc[1]), pk_bf16(acc[2], acc[3]), pk_bf16(acc[4], acc[5]), pk_bf16(acc[6], acc[7])};
    }
    BAR();
    STAMP(9);
    const int rb = lane & 3, rks = (lane >> 2) & 1;
    const bool dvalid = g == ks2;
    if (w < 4) {
      const v4u a = lds_r128(L, L_CCB + rb * 576 + (64 * w + 32 * rks + 8 * g) * 2);
      gemv8_store(a, wvT, L, L_RED2 + ((w * 2 + ks2) * 4 * 64 + 8 * nb8) * 4, 256, dvalid);
    }
    BAR();
    {
      f32x4 a0 = {0.f, 0.f, 0.f, 0.f};
#pragma unroll
      for (int p = 0; p < 8; ++p) a0 += lds_rf4(L, L_RED2 + p * 1024 + lane * 16);
      *(LAS v2u*)(L + L_CX + w * 512 + lane * 8) = v2u{pk_bf16(a0[0], a0[1]), pk_bf16(a0[2], a0[3])};
      asm volatile("s_waitcnt lgkmcnt(0)" ::: "memory");
      const v4u a = lds_r128(L, L_CX + w * 512 + rb * 128 + (32 * rks + 8 * g) * 2);
      gemv8_store(a, woT, L, L_STG + ((w * 2 + ks2) * 4 * 64 + 8 * nb8) * 4, 256, dvalid);
      asm volatile("s_waitcnt lgkmcnt(0)" ::: "memory");
      const f32x4 v = lds_rf4(L, L_STG + w * 2048 + lane * 16) + lds_rf4(L, L_STG + w * 2048 + 1024 + lane * 16);
      const int o = WS_H3S + ((pw * 4 + g) * 512 + 64 * w + 4 * l15) * 8;
      st_sc1(v4u{__float_as_uint(v[0]), ep, __float_as_uint(v[1]), ep}, rs, o);
      st_sc1(v4u{__float_as_uint(v[2]), ep, __float_as_uint(v[3]), ep}, rs, o + 16);
    }
    STAMP(13);
  } else {
    if (ffn && (w == 4 || w == 5)) {
#pragma unroll
      for (int r = 0; r < 8; ++r) wa[r] = ldg(wd + (size_t)(16 * fi + 8 * (g & 1) + r) * 512 + 256 * (w - 4) + 128 * (g >> 1) + 8 * l15);
    }
    const int aj = xcd * 16 + j, ob = aj >> 5, om0 = (aj & 31) * 16;
    v4u wrv[4];
    unsigned xv = 0;
    if (w == 0) {
#pragma unroll
      for (int k = 0; k < 4; ++k) wrv[k] = ldg(wr + l15 * 32 + k * 8);
      xv = *(const GAS unsigned*)(x + ob * 512 + om0 + 2 * (lane & 7));
    }
    PIN();
    const Ln ln = ln_stats(xs);
    if (ffn) {
      constexpr int L_FR = 0, L_HM = 8192, L_FS = 12288;
      if (w < 4) {
        const int rb = lane & 3;
        const float mb = sel4(rb, ln.mb0, ln.mb1, ln.mb2, ln.mb3), den = sel4(rb, ln.d0, ln.d1, ln.d2, ln.d3);
        const v4u a0 = xn_frag(fx0, mb, den), a1 = xn_frag(fx1, mb, den);
        v4u t[8];
        transpose8(wa, t);
        const int ks8 = l15 >> 1;
        const int off = L_FR + ((w * 8 + ks8) * 4 * 16 + 8 * (lane & 1)) * 4;
        gemv8_store(a0, t, L, off, 64, ks8 == g);
        gemv8_store(a1, t, L, off, 64, ks8 == 4 + g);
      }
      BAR();
      if (w == 0) {
        float gs = 0.f, us = 0.f;
#pragma unroll
        for (int p = 0; p < 16; ++p) { gs += *(LAS float*)(L + L_FR + (p * 64 + lane) * 4); us += *(LAS float*)(L + L_FR + ((16 + p) * 64 + lane) * 4); }
        const float gg = rbf(silu_f(rbf(gs)));
        *(LAS u16*)(L + L_HM + lane * 2) = (u16)(pk_bf16(gg * rbf(us), 0.f) & 0xFFFFu);
      }
      BAR();
      if (w == 4 || w == 5) {
        v4u a = lds_r128(L, L_HM + ((lane & 3) * 16 + 8 * (g & 1)) * 2);
        if ((g >> 1) != ((lane >> 2) & 1)) a = v4u{0u, 0u, 0u, 0u};
        v4u t[8];
        transpose8(wa, t);
        const int sb = L_FS + (w - 4) * 4096;
        gemv8_store(a, t, L, sb + (128 * g + 8 * l15) * 4, 1024, g < 2);
        asm volatile("s_waitcnt lgkmcnt(0)" ::: "memory");
#pragma unroll
        for (int bb = 0; bb < 4; ++bb) {
          const f32x4 v = lds_rf4(L, sb + bb * 1024 + lane * 16);
          const int o = WS_H3Y + ((fi * 4 + bb) * 512 + 256 * (w - 4) + 4 * lane) * 8;
          st_sc1(v4u{__float_as_uint(v[0]), ep, __float_as_uint(v[1]), ep}, rs, o);
          st_sc1(v4u{__float_as_uint(v[2]), ep, __float_as_uint(v[3]), ep}, rs, o + 16);
        }
      }
    }
    STAMP(7);
    float rsel = 0.f;
    if (w == 0) {
      const float mb = sel4(g, ln.mb0, ln.mb1, ln.mb2, ln.mb3), den = sel4(g, ln.d0, ln.d1, ln.d2, ln.d3);
      float r = 0.f;
#pragma unroll
      for (int k = 0; k < 4; ++k)
#pragma unroll
        for (int e = 0; e < 4; ++e) { r += xn1(bf_lo(xs[k][e]), mb, den) * bf_lo(wrv[k][e]); r += xn1(bf_hi(xs[k][e]), mb, den) * bf_hi(wrv[k][e]); }
      r = rbf(row16_sum(r));
      r = rbf(1.f / (1.f + __expf(-r)));
      rsel = rdlane(r, 16 * ob);
    }
    {
      constexpr int L_SR = 32768;
      const int piece = tid >> 3, sub = tid & 7;
      const bool yv = piece < 48;
      const int o0s = WS_H3S + ((piece * 4 + ob) * 512 + om0 + 2 * sub) * 8;
      const int oy = WS_H3Y + ((piece * 4 + ob) * 512 + om0 + 2 * sub) * 8;
      const int oy2 = oy + (yv ? 64 * 4 * 512 * 8 : 0);
      while (__builtin_amdgcn_s_memrealtime() - t_s3 < H3_DELAY) __builtin_amdgcn_s_sleep(4);
      v4u a, c, y0, y1;
      for (unsigned spins = 0;; ++spins) {
        asm volatile("" ::: "memory");
        y0 = ld_sc1(rs, oy);
        y1 = ld_sc1(rs, oy2);
        if (__all((y0.y == ep) & (y0.w == ep) & (y1.y == ep) & (y1.w == ep)) || spins > SPIN_CAP) break;
        __builtin_amdgcn_s_sleep(POLL_SLEEP);
      }
      STAMP(8);
      for (unsigned spins = 0;; ++spins) {
        asm volatile("" ::: "memory");
        a = ld_sc1(rs, o0s);
        c = ld_sc1(rs, o0s + 64 * 4 * 512 * 8);
        if (__all((a.y == ep) & (a.w == ep) & (c.y == ep) & (c.w == ep)) || spins > SPIN_CAP) break;
        __builtin_amdgcn_s_sleep(POLL_SLEEP);
      }
      f32x4 v = {__uint_as_float(a.x) + __uint_as_float(c.x), __uint_as_float(a.z) + __uint_as_float(c.z),
                 __uint_as_float(y0.x) + (yv ? __uint_as_float(y1.x) : 0.f), __uint_as_float(y0.z) + (yv ? __uint_as_float(y1.z) : 0.f)};
#pragma unroll
      for (int e = 0; e < 4; ++e) v[e] = x32_sum(x16_sum(v[e] + dpp<0x128>(v[e])));
      if (lane < 8) lds_wf4(L, L_SR + (w * 8 + lane) * 16, v);
      BAR();
      STAMP(9);
      if (w == 0) {
        f32x4 t = {0.f, 0.f, 0.f, 0.f};
#pragma unroll
        for (int k = 0; k < 8; ++k) t += lds_rf4(L, L_SR + (k * 8 + (lane & 7)) * 16);
        if (lane < 8) {
          const float mb = sel4(ob, ln.mb0, ln.mb1, ln.mb2, ln.mb3), den = sel4(ob, ln.d0, ln.d1, ln.d2, ln.d3);
          float o2[2];
#pragma unroll
          for (int e = 0; e < 2; ++e) {
            const float xx = e ? bf_hi(xv) : bf_lo(xv);
            const float yb = rbf(rbf(t[2 + e]) * rsel);
            const float z = rbf(xn1(xx, mb, den) + yb);
            const float gg = rbf(silu_f(z));
            o2[e] = xx + rbf(rbf(t[e]) * gg);
          }
          *(GAS unsigned*)(out + ob * 512 + om0 + 2 * lane) = pk_bf16(o2[0], o2[1]);
        }
        if (aj == 0 && lane == 0) __hip_atomic_store((GAS unsigned*)(ws + WS_EPOCH), ep, RLX_AGENT);
      }
    }
    STAMP(13);
  }
}

extern "C" void kernel_launch(void* const* d_in, const int* in_sizes, int n_in, void* d_out, int out_size, void* d_ws, size_t ws_size,
                              hipStream_t stream) {
  (void)hipFuncSetAttribute((const void*)mla_moe_kernel, hipFuncAttributeMaxDynamicSharedMemorySize, L_TOTAL);
  mla_moe_kernel<<<256, 512, L_TOTAL, stream>>>((const u16*)d_in[0], (const u16*)d_in[1], (const u16*)d_in[2], (const u16*)d_in[3],
                                                (const u16*)d_in[4], (const u16*)d_in[5], (const u16*)d_in[7], (const u16*)d_in[8],
                                                (const u16*)d_in[9], (const u16*)d_in[10], (u16*)d_out, (char*)d_ws);
}
```

```cpp
#include <hip/hip_runtime.h>
#include <hip/hip_bf16.h>
#include <stdint.h>

typedef unsigned short u16;
typedef unsigned v4u __attribute__((ext_vector_type(4)));
typedef unsigned v2u __attribute__((ext_vector_type(2)));
typedef float f32x2 __attribute__((ext_vector_type(2)));
typedef float f32x4 __attribute__((ext_vector_type(4)));
typedef short bf16x8 __attribute__((ext_vector_type(8)));
typedef __bf16 bf2 __attribute__((ext_vector_type(2)));
#define DI __device__ __forceinline__
#define GAS __attribute__((address_space(1)))
#define LAS __attribute__((address_space(3)))
#define RLX_AGENT __ATOMIC_RELAXED, __HIP_MEMORY_SCOPE_AGENT

constexpr int WS_EPOCH = 0;
constexpr int WS_HINT1 = 16384;
constexpr int WS_HINT3 = 32768;
constexpr int WS_HINT2 = 49152;
constexpr int WS_H0 = 65536;
constexpr int WS_H1 = WS_H0 + 512 * 1024;
constexpr int WS_H2S = WS_H1 + 256 * 1024;
constexpr int WS_H3S = WS_H2S + 64 * 1024;
constexpr int WS_H3Y = WS_H3S + 2048 * 1024;
constexpr int WS_F1 = WS_H3Y + 2048 * 1024;
constexpr int WS_H2P = WS_F1 + 1024 * 1024;
constexpr int WS_TOTAL = WS_H2P + 4096 * 1024;
#define STAMPW(k, wv) do {} while (0)
#define STAMP(k) STAMPW(k, 0)
constexpr unsigned SPIN_CAP = 1u << 17;
constexpr unsigned SENT = 0xFFFFFFFFu;
#define H2_DELAY 24
#define H1_DELAY 4
#define W_UK_DELAY 4
#define H0_DELAY 8
#define A_FIRST 4
#define A_SECOND 200
#define H2_WAIT 280
#define NEARLY 2
#define H0_GATE 150
#define H1_GATE 350
#define A_DELAY 0
#define H3_DELAY 16
#define POLL_SLEEP 4

constexpr int L_QA = 131072;
constexpr int L_FR = L_QA;
constexpr int L_CNT = L_QA + 8192;
constexpr int L_MX = 0;
constexpr int L_LS = 1024;
constexpr int L_P = 2048;
constexpr int L_SR = 16384;
constexpr int L_HM = 17408;
constexpr int L_FS = 20480;
constexpr int L_OST = 32768;
constexpr int L_TOTAL = L_QA + 32768;

DI float bf_lo(unsigned u) { return __uint_as_float(u << 16); }
DI float bf_hi(unsigned u) { return __uint_as_float(u & 0xFFFF0000u); }
DI unsigned pk_bf16(float lo, float hi) { f32x2 v = {lo, hi}; return __builtin_bit_cast(unsigned, __builtin_convertvector(v, bf2)); }
DI float rbf(float f) { return bf_lo(pk_bf16(f, f)); }
DI f32x4 mfma16(v4u a, v4u b, f32x4 c) { return __builtin_amdgcn_mfma_f32_16x16x32_bf16(__builtin_bit_cast(bf16x8, a), __builtin_bit_cast(bf16x8, b), c, 0, 0, 0); }
template <int CTRL> DI float dpp(float v) { return __builtin_bit_cast(float, __builtin_amdgcn_update_dpp(0, __builtin_bit_cast(int, v), CTRL, 0xF, 0xF, true)); }
DI float row16_sum(float v) { v += dpp<0xB1>(v); v += dpp<0x4E>(v); v += dpp<0x141>(v); v += dpp<0x140>(v); return v; }
DI float row16_max(float v) { v = fmaxf(v, dpp<0xB1>(v)); v = fmaxf(v, dpp<0x4E>(v)); v = fmaxf(v, dpp<0x141>(v)); v = fmaxf(v, dpp<0x140>(v)); return v; }
DI float rdlane(float v, int l) { return __builtin_bit_cast(float, __builtin_amdgcn_readlane(__builtin_bit_cast(int, v), l)); }
DI void x32_both(float v, float& lo, float& hi) {
  unsigned a = __builtin_bit_cast(unsigned, v), b = a;
  asm volatile("" : "+v"(b));
  auto r = __builtin_amdgcn_permlane32_swap(a, b, false, false);
  lo = __builtin_bit_cast(float, (unsigned)r[0]); hi = __builtin_bit_cast(float, (unsigned)r[1]);
}
DI void x16_both(float v, float& lo, float& hi) {
  unsigned a = __builtin_bit_cast(unsigned, v), b = a;
  asm volatile("" : "+v"(b));
  auto r = __builtin_amdgcn_permlane16_swap(a, b, false, false);
  lo = __builtin_bit_cast(float, (unsigned)r[0]); hi = __builtin_bit_cast(float, (unsigned)r[1]);
}
DI float x32_sum(float v) { float a, b; x32_both(v, a, b); return a + b; }
DI float x32_max(float v) { float a, b; x32_both(v, a, b); return fmaxf(a, b); }
DI float x16_sum(float v) { float a, b; x16_both(v, a, b); return a + b; }
DI float x16_max(float v) { float a, b; x16_both(v, a, b); return fmaxf(a, b); }
DI v4u ldg(const u16* p) { return __builtin_nontemporal_load((const GAS v4u*)p); }
DI v4u ldx(const u16* p) { return *(const GAS v4u*)p; }
DI v4u ld_sc1(__amdgpu_buffer_rsrc_t rs, int off) { return __builtin_amdgcn_raw_buffer_load_b128(rs, off, 0, 16); }
DI void st_sc1(v4u v, __amdgpu_buffer_rsrc_t rs, int off) { __builtin_amdgcn_raw_buffer_store_b128(v, rs, off, 0, 16); }
#define BAR() do { asm volatile("s_waitcnt lgkmcnt(0)" ::: "memory"); __builtin_amdgcn_s_barrier(); asm volatile("" ::: "memory"); } while (0)
#define PIN() do { asm volatile("" ::: "memory"); __builtin_amdgcn_sched_barrier(0); } while (0)
#define VM0() asm volatile("s_waitcnt vmcnt(0)" ::: "memory")
DI v4u lds_r128(LAS char* L, int off) { return *(LAS v4u*)(L + off); }
DI f32x4 lds_rf4(LAS char* L, int off) { return *(LAS f32x4*)(L + off); }
DI void lds_wf4(LAS char* L, int off, f32x4 v) { *(LAS f32x4*)(L + off) = v; }

DI void transpose8(const v4u (&r)[8], v4u (&t)[8]) {
#pragma unroll
  for (int i = 0; i < 8; ++i)
#pragma unroll
    for (int p = 0; p < 4; ++p)
      t[i][p] = __builtin_amdgcn_perm(r[2 * p + 1][i >> 1], r[2 * p][i >> 1], (i & 1) ? 0x07060302u : 0x05040100u);
}
DI void gemv8_store(v4u a, const v4u (&t)[8], LAS char* L, int off, int bstride, bool valid) {
  f32x4 d[8];
#pragma unroll
  for (int i = 0; i < 8; ++i) d[i] = mfma16(a, t[i], f32x4{0.f, 0.f, 0.f, 0.f});
  if (valid) {
#pragma unroll
    for (int bb = 0; bb < 4; ++bb) {
      lds_wf4(L, off + bb * bstride, f32x4{d[0][bb], d[1][bb], d[2][bb], d[3][bb]});
      lds_wf4(L, off + bb * bstride + 16, f32x4{d[4][bb], d[5][bb], d[6][bb], d[7][bb]});
    }
  }
}

struct Ln { float mb0, mb1, mb2, mb3, d0, d1, d2, d3; };
DI Ln ln_stats(const v4u (&xs)[4]) {
  float s = 0.f;
#pragma unroll
  for (int k = 0; k < 4; ++k)
#pragma unroll
    for (int e = 0; e < 4; ++e) s += bf_lo(xs[k][e]) + bf_hi(xs[k][e]);
  const float mean = row16_sum(s) * (1.f / 512.f);
  float q = 0.f;
#pragma unroll
  for (int k = 0; k < 4; ++k)
#pragma unroll
    for (int e = 0; e < 4; ++e) { const float a = bf_lo(xs[k][e]) - mean, b = bf_hi(xs[k][e]) - mean; q += a * a; q += b * b; }
  const float var = rbf(row16_sum(q) * (1.f / 512.f));
  const float mb = rbf(mean), den = 1.f / rbf(sqrtf(rbf(var + 1e-5f)));
  Ln o;
  o.mb0 = rdlane(mb, 0); o.mb1 = rdlane(mb, 16); o.mb2 = rdlane(mb, 32); o.mb3 = rdlane(mb, 48);
  o.d0 = rdlane(den, 0); o.d1 = rdlane(den, 16); o.d2 = rdlane(den, 32); o.d3 = rdlane(den, 48);
  return o;
}
DI float sel4(int b, float a0, float a1, float a2, float a3) { const float lo = (b & 1) ? a1 : a0, hi = (b & 1) ? a3 : a2; return (b & 2) ? hi : lo; }
DI float xn1(float x, float mb, float rden) { return rbf(rbf(x - mb) * rden); }
DI v4u xn_frag(v4u x, float mb, float rden) {
  v4u o;
#pragma unroll
  for (int e = 0; e < 4; ++e) o[e] = pk_bf16(rbf(bf_lo(x[e]) - mb) * rden, rbf(bf_hi(x[e]) - mb) * rden);
  return o;
}
DI unsigned swz(unsigned row) { return ((row & 3) << 2) | ((row >> 2) & 3); }
DI float silu_f(float z) { return z / (1.f + __expf(-z)); }

#define LCNT_ADD(o) do { asm volatile("s_waitcnt lgkmcnt(0)" ::: "memory"); if (lane == 0) __hip_atomic_fetch_add((LAS unsigned*)(L + (o)), 1u, __ATOMIC_RELAXED, __HIP_MEMORY_SCOPE_WORKGROUP); } while (0)
#define LCNT_WAIT(o, n) do { while (__builtin_amdgcn_readfirstlane(__hip_atomic_load((LAS unsigned*)(L + (o)), __ATOMIC_RELAXED, __HIP_MEMORY_SCOPE_WORKGROUP)) < (n)) {} asm volatile("" ::: "memory"); } while (0)

#define DUMP() do {} while (0)
__global__ __launch_bounds__(512, 2) void mla_moe_kernel(const u16* x, const u16* ckv, const u16* wq, const u16* wuk, const u16* wuv,
    const u16* wo, const u16* wr, const u16* wg, const u16* wu, const u16* wd, u16* out, char* ws) {
  extern __shared__ __attribute__((aligned(16))) char smem[];
  LAS char* L = (LAS char*)smem;
  {
    unsigned long long pc0;
    asm volatile("s_getpc_b64 %0" : "=s"(pc0));
    const unsigned voff = threadIdx.x * 16;
    const unsigned ldst = (unsigned)(size_t)L + 131072 + 8192;
    unsigned keep;
    asm volatile("s_mov_b32 %0, m0\n\ts_mov_b32 m0, %3\n\ts_nop 0\n\t"
                 "global_load_lds_dwordx4 %1, %2\n\t"
                 "global_load_lds_dwordx4 %1, %4\n\t"
                 "global_load_lds_dwordx4 %1, %5\n\t"
                 "s_mov_b32 m0, %0"
                 : "=&s"(keep) : "v"(voff), "s"(pc0 & ~255ull), "s"(ldst), "s"((pc0 & ~255ull) + 8192), "s"((pc0 & ~255ull) + 16384) : "memory");
  }
  const int tid = threadIdx.x, lane = tid & 63, w = __builtin_amdgcn_readfirstlane(tid >> 6);
  const int xcd = blockIdx.x & 7, idx = blockIdx.x >> 3;
  const bool roleW = idx < 16;
  const int j = idx & 15;
  const int g = lane >> 4, l15 = lane & 15;
  STAMP(0);
  const unsigned long long t_start = __builtin_amdgcn_s_memrealtime();
  const __amdgpu_buffer_rsrc_t rs = __builtin_amdgcn_make_buffer_rsrc(ws, 0, WS_TOTAL, 0x00020000);

  if (__builtin_expect(roleW, 1)) {
    constexpr int L_RED = 0, L_QS = 8192, L_Q2 = 9216, L_CNTW = 10240, L_CCB = 12288, L_RED2 = 17408, L_CX = 26624, L_STG = 36864, L_FRW = 53248, L_WK = 65536;
    const int h = xcd * 4 + (j >> 2), j4 = j & 3, ch = j4 >> 1, dh = j4 & 1, pw = xcd * 16 + j;
    const int fgrp = xcd * 2 + (j >> 3), fkq = j & 7, fi = fgrp * 8 + fkq;
    const bool ffn = fgrp < 14;
    const unsigned ep_v = __hip_atomic_load((GAS unsigned*)(ws + WS_EPOCH), RLX_AGENT);
    unsigned ep;
    const int ks2 = (lane >> 3) & 1, nb8 = lane & 7;
    v4u wa[8], wb[8];
#define LOAD_WO() _Pragma("unroll") for (int r = 0; r < 8; ++r) wb[r] = ldg(wo + (size_t)(h * 128 + 64 * dh + 32 * ks2 + 8 * g + r) * 512 + 64 * w + 8 * nb8)
    if (__builtin_expect(w >= 4, 1)) {
      const int m0 = 128 * j4 + 32 * (w - 4);
      v4u wt[8], xs[4];
#pragma unroll
      for (int k = 0; k < 4; ++k) xs[k] = ldx(x + g * 512 + l15 * 32 + k * 8);
      const v4u xf = ldx(x + (lane & 3) * 512 + m0 + 8 * g);
      const int k0 = 64 * fkq + 32 * (w & 1);
      const v4u fx = ldx(x + (lane & 3) * 512 + k0 + 8 * g);
#pragma unroll
      for (int r = 0; r < 8; ++r) wt[r] = ldg(wq + (size_t)(m0 + 8 * g + r) * 4096 + h * 128 + 8 * l15);
      PIN();
      STAMP(3);
      const Ln ln = ln_stats(xs);
      ep = __builtin_amdgcn_readfirstlane(ep_v) + 1u;
      const int rb = lane & 3;
      const float mb = sel4(rb, ln.mb0, ln.mb1, ln.mb2, ln.mb3), den = sel4(rb, ln.d0, ln.d1, ln.d2, ln.d3);
      const v4u a = xn_frag(xf, mb, den), a0 = xn_frag(fx, mb, den);
      PIN();
      STAMP(5);
      v4u t[8];
      transpose8(wt, t);
      gemv8_store(a, t, L, L_RED + ((w - 4) * 4 * 128 + 8 * lane) * 4, 512, lane < 16);
      STAMP(11);
      BAR();
      BAR();
      BAR();
      if (ffn) {
        const u16* wsrc = (w < 6 ? wg : wu) + (size_t)(k0 + 8 * g) * 1792 + 128 * fgrp + 8 * l15;
        v4u wf[8];
#pragma unroll
        for (int r = 0; r < 8; ++r) wf[r] = ldg(wsrc + (size_t)r * 1792);
        PIN();
        LOAD_WO();
        PIN();
        transpose8(wf, t);
        gemv8_store(a0, t, L, L_FRW + ((w - 4) * 512 + 8 * lane) * 4, 512, lane < 16);
        LCNT_ADD(L_CNTW);
        LCNT_WAIT(L_CNTW, 4u);
#pragma unroll
        for (int k = 0; k < 2; ++k) {
          const int mat = (w - 4) >> 1, bb = 2 * (w & 1) + k;
          const f32x2 v = *(LAS f32x2*)(L + L_FRW + ((2 * mat) * 512 + bb * 128 + 2 * lane) * 4) + *(LAS f32x2*)(L + L_FRW + ((2 * mat + 1) * 512 + bb * 128 + 2 * lane) * 4);
          st_sc1(v4u{__float_as_uint(v.x), ep, __float_as_uint(v.y), ep}, rs, WS_F1 + (((fi * 2 + mat) * 4 + bb) * 128 + 2 * lane) * 8);
        }
        PIN();
      } else {
        LOAD_WO();
        PIN();
      }
    } else {
      const int c0 = 128 * j4 + 32 * w;
      if (tid == 0) *(LAS v2u*)(L + L_CNTW) = v2u{0u, 0u};
      ep = __builtin_amdgcn_readfirstlane(ep_v) + 1u;
      PIN();
#pragma unroll
      for (int i = 0; i < 8; ++i) {
        const int row = 4 * i + g;
        __builtin_amdgcn_global_load_lds((const GAS unsigned*)(wuk + (size_t)(c0 + row) * 4096 + h * 128 + (l15 ^ (row & 15)) * 8),
                                         (LAS unsigned*)(L + L_WK + w * 8192 + i * 1024), 16, 0, 2);
      }
      PIN();
      BAR();
      STAMP(1);
      {
        f32x2 s = {0.f, 0.f};
#pragma unroll
        for (int k = 0; k < 4; ++k) s += *(LAS f32x2*)(L + L_RED + ((k * 4 + w) * 128 + 2 * lane) * 4);
        st_sc1(v4u{__float_as_uint(s.x), ep, __float_as_uint(s.y), ep}, rs, WS_H0 + (((h * 4 + j4) * 4 + w) * 64 + lane) * 16);
      }
      PIN();
      BAR();
      asm volatile("s_waitcnt vmcnt(1)" ::: "memory");
      v4u wk[8];
#pragma unroll
      for (int rt = 0; rt < 2; ++rt)
#pragma unroll
        for (int ks = 0; ks < 4; ++ks) wk[rt * 4 + ks] = lds_r128(L, L_WK + w * 8192 + (16 * rt + l15) * 256 + 16 * ((4 * ks + g) ^ l15));
      __builtin_amdgcn_s_sleep(H0_DELAY);
      v4u v[4];
      for (unsigned spins = 0;; ++spins) {
        asm volatile("" ::: "memory");
        bool ok = true;
#pragma unroll
        for (int mq = 0; mq < 4; ++mq) v[mq] = ld_sc1(rs, WS_H0 + (((h * 4 + mq) * 4 + w) * 64 + lane) * 16);
#pragma unroll
        for (int mq = 0; mq < 4; ++mq) ok &= (v[mq].y == ep) & (v[mq].w == ep);
        if (__all(ok) || spins > SPIN_CAP) break;
        __builtin_amdgcn_s_sleep(2);
      }
      if (tid == 0) st_sc1(v4u{ep, ep, ep, ep}, rs, WS_HINT1 + pw * 128);
      STAMP(12);
      const float q0 = (__uint_as_float(v[0].x) + __uint_as_float(v[1].x)) + (__uint_as_float(v[2].x) + __uint_as_float(v[3].x));
      const float q1 = (__uint_as_float(v[0].z) + __uint_as_float(v[1].z)) + (__uint_as_float(v[2].z) + __uint_as_float(v[3].z));
      *(LAS unsigned*)(L + L_QS + (w * 128 + 2 * lane) * 2) = pk_bf16(q0, q1);
      LCNT_ADD(L_CNTW + 4);
      LCNT_WAIT(L_CNTW + 4, 4u);
      v4u bq[4];
#pragma unroll
      for (int ks = 0; ks < 4; ++ks) bq[ks] = lds_r128(L, L_QS + ((lane & 3) * 128 + 32 * ks + 8 * g) * 2);
#pragma unroll
      for (int rt = 0; rt < 2; ++rt) {
        f32x4 d = {0.f, 0.f, 0.f, 0.f};
#pragma unroll
        for (int ks = 0; ks < 4; ++ks) d = mfma16(wk[rt * 4 + ks], bq[ks], d);
        if (l15 < 4) *(LAS v2u*)(L + L_Q2 + w * 256 + l15 * 64 + (4 * rt + g) * 8) = v2u{pk_bf16(d[0], d[1]), pk_bf16(d[2], d[3])};
      }
      asm volatile("s_waitcnt lgkmcnt(0)" ::: "memory");
      if (lane < 32) {
        const v2u d = *(LAS v2u*)(L + L_Q2 + w * 256 + lane * 8);
        st_sc1(v4u{d.x, ep, d.y, ep}, rs, WS_H1 + (((lane >> 3) * 32 + h) * 128 + (c0 >> 2) + (lane & 7)) * 16);
      }
      STAMP(14);
      PIN();
      BAR();
      __builtin_amdgcn_s_sleep(2);
#pragma unroll
      for (int r = 0; r < 8; ++r) wa[r] = ldg(wuv + (size_t)(256 * ch + 64 * w + 32 * ks2 + 8 * g + r) * 4096 + h * 128 + 64 * dh + 8 * nb8);
      LOAD_WO();
      PIN();
    }
    STAMP(6);
    v4u wvT[8], woT[8];
    {
      if (w < 4) transpose8(wa, wvT);
      transpose8(wb, woT);
#pragma unroll
      for (int i = 0; i < 8; ++i) asm volatile("" : "+v"(woT[i]), "+v"(wvT[i]));
    }
    STAMP(7);
    const int bb = w >> 1;
    {
      {
        v4u t1;
        for (unsigned spins = 0; spins <= SPIN_CAP; ++spins) {
          asm volatile("" ::: "memory");
          t1 = ld_sc1(rs, WS_HINT2 + (bb * 32 + (lane & 31)) * 128);
          if (__all((t1.x == ep) & (t1.z == ep))) break;
          __builtin_amdgcn_s_sleep(POLL_SLEEP);
        }
        while (__any((unsigned)__builtin_amdgcn_s_memrealtime() - t1.y < H2_WAIT)) __builtin_amdgcn_s_sleep(1);
      }
      STAMP(8);
      v4u p[8], s;
      const int base = WS_H2P + (((bb * 32 + g) * 32 + h) * 512 + 256 * ch + 128 * (w & 1) + 8 * l15) * 2;
      for (unsigned spins = 0;; ++spins) {
        asm volatile("" ::: "memory");
        s = ld_sc1(rs, WS_H2S + ((bb * 32 + (lane & 31)) * 32 + h) * 16);
#pragma unroll
        for (int i = 0; i < 8; ++i) p[i] = ld_sc1(rs, base + (i ^ (4 * dh)) * (4 * 32 * 512 * 2));
        bool ok = (s.y == ep) & (s.w == ep);
#pragma unroll
        for (int i = 0; i < 8; ++i) ok &= (p[i].x != SENT) & (p[i].y != SENT) & (p[i].z != SENT) & (p[i].w != SENT);
        const bool allok = __all(ok);
        if (spins == 0) STAMP(2);
        if (allok || spins > SPIN_CAP) break;
        __builtin_amdgcn_s_sleep(2);
      }
      float wgt;
      {
        const float m = __uint_as_float(s.x), l = __uint_as_float(s.z);
        const float Mx = x16_max(row16_max(m));
        const float e = __builtin_amdgcn_exp2f(m - Mx);
        wgt = e * __builtin_amdgcn_rcpf(x16_sum(row16_sum(e * l)));
      }
      if (tid == 0) st_sc1(v4u{ep, ep, ep, ep}, rs, WS_HINT3 + pw * 128);
      STAMP(15);
      float acc[8] = {0.f, 0.f, 0.f, 0.f, 0.f, 0.f, 0.f, 0.f};
#pragma unroll
      for (int i = 0; i < 8; ++i) {
        const int s0 = 4 * (i ^ (4 * dh));
        const float ww = sel4(g, rdlane(wgt, s0), rdlane(wgt, s0 + 1), rdlane(wgt, s0 + 2), rdlane(wgt, s0 + 3));
#pragma unroll
        for (int e = 0; e < 4; ++e) { acc[2 * e] += ww * bf_lo(p[i][e]); acc[2 * e + 1] += ww * bf_hi(p[i][e]); }
      }
#pragma unroll
      for (int e = 0; e < 8; ++e) acc[e] = x32_sum(x16_sum(acc[e]));
      if (lane < 16)
        *(LAS v4u*)(L + L_CCB + bb * 576 + (128 * (w & 1) + 8 * lane) * 2) =
            v4u{pk_bf16(acc[0], acc[1]), pk_bf16(acc[2], acc[3]), pk_bf16(acc[4], acc[5]), pk_bf16(acc[6], acc[7])};
    }
    BAR();
    STAMP(9);
    const int rb = lane & 3, rks = (lane >> 2) & 1;
    const bool dvalid = g == ks2;
    if (w < 4) {
      const v4u a = lds_r128(L, L_CCB + rb * 576 + (64 * w + 32 * rks + 8 * g) * 2);
      gemv8_store(a, wvT, L, L_RED2 + ((w * 2 + ks2) * 4 * 64 + 8 * nb8) * 4, 256, dvalid);
    }
    BAR();
    {
      f32x4 a0 = {0.f, 0.f, 0.f, 0.f};
#pragma unroll
      for (int p = 0; p < 8; ++p) a0 += lds_rf4(L, L_RED2 + p * 1024 + lane * 16);
      *(LAS v2u*)(L + L_CX + w * 512 + lane * 8) = v2u{pk_bf16(a0[0], a0[1]), pk_bf16(a0[2], a0[3])};
      asm volatile("s_waitcnt lgkmcnt(0)" ::: "memory");
      const v4u a = lds_r128(L, L_CX + w * 512 + rb * 128 + (32 * rks + 8 * g) * 2);
      gemv8_store(a, woT, L, L_STG + ((w * 2 + ks2) * 4 * 64 + 8 * nb8) * 4, 256, dvalid);
      asm volatile("s_waitcnt lgkmcnt(0)" ::: "memory");
#pragma unroll
      for (int k = 0; k < 2; ++k) {
        const f32x2 v = *(LAS f32x2*)(L + L_STG + w * 2048 + k * 512 + lane * 8) + *(LAS f32x2*)(L + L_STG + w * 2048 + 1024 + k * 512 + lane * 8);
        st_sc1(v4u{__float_as_uint(v.x), ep, __float_as_uint(v.y), ep}, rs, WS_H3S + ((pw * 4 + 2 * k + (lane >> 5)) * 512 + 64 * w + 2 * (lane & 31)) * 8);
      }
    }
    STAMP(13);
      DUMP();
    return;
  }
  {
    const int b = xcd >> 1, slice = (xcd & 1) * 16 + j;
    const int fgrp = xcd * 2 + (j >> 3), fkq = j & 7, fi = fgrp * 8 + fkq;
    const bool ffn = fgrp < 14;
    unsigned ep_v;
    asm volatile("global_load_dword %0, %1, off sc1" : "=v"(ep_v) : "v"(ws + WS_EPOCH) : "memory");
    while (__builtin_amdgcn_s_memrealtime() - t_start < A_DELAY) __builtin_amdgcn_s_sleep(1);
    PIN();
    {
      const u16* ksrc = ckv + ((size_t)(b * 4096 + slice * 128 + 16 * w)) * 512 + g * 128;
#pragma unroll
      for (int i = 0; i < 16; ++i) {
        if (i == A_FIRST) { PIN(); while (__builtin_amdgcn_s_memrealtime() - t_start < A_SECOND) __builtin_amdgcn_s_sleep(1); PIN(); }
        __builtin_amdgcn_global_load_lds((const GAS unsigned*)(ksrc + (size_t)i * 512 + (l15 ^ swz(i)) * 8),
                                         (LAS unsigned*)(L + (16 * w + i) * 1024), 16, 0, 2);
      }
    }
    PIN();
    __builtin_amdgcn_s_barrier();
    const int pofs = WS_H2P + (((b * 32 + slice) * 32 + (lane >> 3)) * 512 + 64 * w + 8 * (lane & 7)) * 2;
#pragma unroll
    for (int i = 0; i < 4; ++i) st_sc1(v4u{SENT, SENT, SENT, SENT}, rs, pofs + i * 8192);
    PIN();
    asm volatile("s_waitcnt vmcnt(4)" : "+v"(ep_v) :: "memory");
    const unsigned ep = __builtin_amdgcn_readfirstlane(ep_v) + 1u;
    v4u ka[16];
    v2u tr[4][4][2];
    const unsigned lb = (unsigned)(size_t)L;
    {
      const int krow = 8 * ((l15 >> 2) & 1) + 4 * (l15 >> 3) + (l15 & 3);
      const int sk = swz(krow);
#pragma unroll
      for (int k4 = 0; k4 < 4; ++k4) {
        const unsigned addr = lb + (16 * w + krow) * 1024 + 16 * ((4 * k4 + g) ^ sk);
#pragma unroll
        for (int ct = 0; ct < 4; ++ct) asm volatile("ds_read_b128 %0, %1 offset:%2" : "=v"(ka[ct * 4 + k4]) : "v"(addr), "i"(ct * 256) : "memory");
      }
    }
    BAR();
    STAMP(10);
    {
      const int q4 = l15 >> 2, p4 = lane & 3;
      const unsigned row = 8 * (g & 1) + 4 * (g >> 1) + q4;
#pragma unroll
      for (int u = 0; u < 2; ++u) {
        const unsigned addr = lb + row * 1024 + (w >> 1) * 256 + 16 * ((8 * (w & 1) + 4 * u + p4) ^ swz(row));
#pragma unroll
        for (int s = 0; s < 4; ++s)
#pragma unroll
          for (int t2 = 0; t2 < 2; ++t2)
#pragma unroll
            for (int e = 0; e < 2; ++e)
              asm volatile("ds_read_b64_tr_b16 %0, %1 offset:%2" : "=v"(tr[s][u * 2 + e][t2]) : "v"(addr + (s >> 1) * 65536), "i"((s & 1) * 32768 + t2 * 16384 + 8 * e) : "memory");
      }
    }
    {
      const int qbase = WS_H1 + ((b * 32 + 4 * w) * 128 + lane) * 16;
      for (unsigned spins = 0; spins <= SPIN_CAP; ++spins) {
        asm volatile("" ::: "memory");
        const v4u t1 = ld_sc1(rs, WS_HINT1 + (16 * w + l15) * 128);
        if (__all((t1.x == ep) & (t1.z == ep))) { if (spins) __builtin_amdgcn_s_sleep(H1_DELAY); break; }
        __builtin_amdgcn_s_sleep(POLL_SLEEP);
      }
      STAMP(2);
      v4u qv[8];
      for (unsigned spins = 0;; ++spins) {
        asm volatile("" ::: "memory");
#pragma unroll
        for (int i = 0; i < 8; ++i) qv[i] = ld_sc1(rs, qbase + i * 1024);
        bool ok = true;
#pragma unroll
        for (int i = 0; i < 8; ++i) ok &= (qv[i].y == ep) & (qv[i].w == ep);
        if (__all(ok) || spins > SPIN_CAP) break;
        __builtin_amdgcn_s_sleep(2);
      }
      STAMP(11);
      const int gq = (lane >> 1) & 3;
#pragma unroll
      for (int i = 0; i < 8; ++i) {
        const int h16 = (4 * w + (i >> 1)) & 15, ks = 8 * (i & 1) + (lane >> 3);
        *(LAS v2u*)(L + L_QA + (w >> 2) * 16384 + ks * 1024 + ((h16 ^ (gq | ((ks & 1) << 2))) + 16 * gq) * 16 + 8 * (lane & 1)) = v2u{qv[i].x, qv[i].z};
      }
    }
    VM0();
    BAR();
#pragma unroll
    for (int s = 0; s < 4; ++s) {
      asm volatile("" : "+v"(tr[s][0][0]), "+v"(tr[s][0][1]), "+v"(tr[s][1][0]), "+v"(tr[s][1][1]), "+v"(tr[s][2][0]), "+v"(tr[s][2][1]), "+v"(tr[s][3][0]), "+v"(tr[s][3][1]));
      asm volatile("" : "+v"(ka[4 * s]), "+v"(ka[4 * s + 1]), "+v"(ka[4 * s + 2]), "+v"(ka[4 * s + 3]));
    }
    if (tid == 0) st_sc1(v4u{ep, (unsigned)__builtin_amdgcn_s_memrealtime(), ep, 0u}, rs, WS_HINT2 + (b * 32 + slice) * 128);
    STAMP(3);
    float sc[2][4];
    {
      constexpr int RING = 16;
      v4u bq[RING];
      const unsigned qa0 = (unsigned)(size_t)L + L_QA + ((l15 ^ g) + 16 * g) * 16, qa1 = (unsigned)(size_t)L + L_QA + ((l15 ^ (g | 4)) + 16 * g) * 16;
#define QREAD(i) asm volatile("ds_read_b128 %0, %1 offset:%2" : "=v"(bq[(i) % RING]) : "v"((((i) >> 1) & 1) ? qa1 : qa0), "i"(((i) & 1) * 16384 + ((i) >> 1) * 1024) : "memory")
#pragma unroll
      for (int i = 0; i < RING; ++i) QREAD(i);
      f32x4 acc[2] = {{0.f, 0.f, 0.f, 0.f}, {0.f, 0.f, 0.f, 0.f}};
#pragma unroll
      for (int i = 0; i < 32; i += 2) {
        asm volatile("s_waitcnt lgkmcnt(%2)" : "+v"(bq[i % RING]), "+v"(bq[(i + 1) % RING]) : "i"(i + RING <= 32 ? RING - 2 : 30 - i) : "memory");
        acc[0] = mfma16(ka[i >> 1], bq[i % RING], acc[0]);
        acc[1] = mfma16(ka[i >> 1], bq[(i + 1) % RING], acc[1]);
        __builtin_amdgcn_sched_barrier(0);
        if (i + RING < 32) { QREAD(i + RING); QREAD(i + RING + 1); }
      }
      constexpr float SC = 0.08838834764831845f * 1.4426950408889634f;
      float m[2];
#pragma unroll
      for (int hh = 0; hh < 2; ++hh) {
#pragma unroll
        for (int e = 0; e < 4; ++e) sc[hh][e] = acc[hh][e] * SC;
        m[hh] = fmaxf(fmaxf(sc[hh][0], sc[hh][1]), fmaxf(sc[hh][2], sc[hh][3]));
      }
      auto r1 = __builtin_amdgcn_permlane16_swap(__float_as_uint(m[0]), __float_as_uint(m[1]), false, false);
      const float mm = x32_max(fmaxf(__uint_as_float(r1[0]), __uint_as_float(r1[1])));
      if (lane < 32) *(LAS float*)(L + L_MX + (lane * 8 + w) * 4) = mm;
    }
    BAR();
    STAMP(4);
    float M[2];
    {
      f32x4 mx[2][2];
#pragma unroll
      for (int hh = 0; hh < 2; ++hh) { mx[hh][0] = lds_rf4(L, L_MX + (hh * 16 + l15) * 32); mx[hh][1] = lds_rf4(L, L_MX + (hh * 16 + l15) * 32 + 16); }
#pragma unroll
      for (int hh = 0; hh < 2; ++hh) {
        M[hh] = fmaxf(fmaxf(fmaxf(mx[hh][0][0], mx[hh][0][1]), fmaxf(mx[hh][0][2], mx[hh][0][3])), fmaxf(fmaxf(mx[hh][1][0], mx[hh][1][1]), fmaxf(mx[hh][1][2], mx[hh][1][3])));
        const v2u p = {pk_bf16(__builtin_amdgcn_exp2f(sc[hh][0] - M[hh]), __builtin_amdgcn_exp2f(sc[hh][1] - M[hh])),
                       pk_bf16(__builtin_amdgcn_exp2f(sc[hh][2] - M[hh]), __builtin_amdgcn_exp2f(sc[hh][3] - M[hh]))};
        *(LAS v2u*)(L + L_P + hh * 4096 + ((w >> 1) * 64 + lane) * 16 + 8 * (w & 1)) = p;
      }
    }
    BAR();
    STAMP(5);
    {
      v4u pb[2][4];
#pragma unroll
      for (int hh = 0; hh < 2; ++hh)
#pragma unroll
        for (int s = 0; s < 4; ++s) pb[hh][s] = lds_r128(L, L_P + hh * 4096 + (s * 64 + lane) * 16);
      if (w == 0) {
        const v4u ones = {0x3F803F80u, 0x3F803F80u, 0x3F803F80u, 0x3F803F80u};
        f32x4 l0 = {0.f, 0.f, 0.f, 0.f}, l1 = {0.f, 0.f, 0.f, 0.f};
#pragma unroll
        for (int s = 0; s < 4; ++s) { l0 = mfma16(ones, pb[0][s], l0); l1 = mfma16(ones, pb[1][s], l1); }
        if (lane < 32)
          st_sc1(v4u{__float_as_uint(lane < 16 ? M[0] : M[1]), ep, __float_as_uint(lane < 16 ? l0[0] : l1[0]), ep}, rs, WS_H2S + ((b * 32 + slice) * 32 + lane) * 16);
      }
#pragma unroll
      for (int hh = 0; hh < 2; ++hh) {
        f32x4 o[4];
#pragma unroll
        for (int c = 0; c < 4; ++c) o[c] = f32x4{0.f, 0.f, 0.f, 0.f};
#pragma unroll
        for (int s = 0; s < 4; ++s)
#pragma unroll
          for (int c = 0; c < 4; ++c) o[c] = mfma16(v4u{tr[s][c][0].x, tr[s][c][0].y, tr[s][c][1].x, tr[s][c][1].y}, pb[hh][s], o[c]);
#pragma unroll
        for (int u = 0; u < 2; ++u) {
          v4u v = {pk_bf16(o[2 * u][0], o[2 * u][1]), pk_bf16(o[2 * u][2], o[2 * u][3]), pk_bf16(o[2 * u + 1][0], o[2 * u + 1][1]), pk_bf16(o[2 * u + 1][2], o[2 * u + 1][3])};
#pragma unroll
          for (int e = 0; e < 4; ++e) v[e] = v[e] == SENT ? 0x7FC07FC0u : v[e];
          *(LAS v4u*)(L + L_OST + w * 4096 + (hh * 16 + l15) * 128 + 16 * ((4 * u + g) ^ (l15 & 7))) = v;
        }
#pragma unroll
        for (int i = 2 * hh; i < 2 * hh + 2; ++i) {
          const int hr = 8 * i + (lane >> 3);
          st_sc1(lds_r128(L, L_OST + w * 4096 + hr * 128 + 16 * ((lane & 7) ^ (hr & 7))), rs, pofs + i * 8192);
        }
      }
    }
    PIN();
    __builtin_amdgcn_s_barrier();
    STAMP(6);
    if (ffn && (w == 1 || w == 2)) {
      v4u wf[8];
      const int wd2 = w - 1;
#pragma unroll
      for (int r = 0; r < 8; ++r) wf[r] = ldg(wd + (size_t)(16 * fi + 8 * (g & 1) + r) * 512 + 256 * wd2 + 128 * (g >> 1) + 8 * l15);
      v4u gv[8];
      const int gofs = WS_F1 + ((fgrp * 8 * 8 + (lane >> 3)) * 128 + 16 * fkq + 2 * (lane & 7)) * 8;
      for (unsigned spins = 0;; ++spins) {
        asm volatile("" ::: "memory");
        bool ok = true;
#pragma unroll
        for (int k = 0; k < 8; ++k) gv[k] = ld_sc1(rs, gofs + k * 8192);
#pragma unroll
        for (int k = 0; k < 8; ++k) ok &= (gv[k].y == ep) & (gv[k].w == ep);
        if (__all(ok) || spins > SPIN_CAP) break;
        __builtin_amdgcn_s_sleep(8);
      }
      float s0 = 0.f, s1 = 0.f;
#pragma unroll
      for (int k = 0; k < 8; ++k) { s0 += __uint_as_float(gv[k].x); s1 += __uint_as_float(gv[k].z); }
      s0 = rbf(s0); s1 = rbf(s1);
      float g0, u0, g1, u1;
      x32_both(s0, g0, u0);
      x32_both(s1, g1, u1);
      if (lane < 32) *(LAS unsigned*)(L + L_HM + wd2 * 128 + lane * 4) = pk_bf16(rbf(silu_f(g0)) * u0, rbf(silu_f(g1)) * u1);
      asm volatile("s_waitcnt lgkmcnt(0)" ::: "memory");
      v4u a = lds_r128(L, L_HM + wd2 * 128 + ((lane & 3) * 16 + 8 * (g & 1)) * 2);
      if ((g >> 1) != ((lane >> 2) & 1)) a = v4u{0u, 0u, 0u, 0u};
      v4u t[8];
      transpose8(wf, t);
      const int sb = L_FS + wd2 * 4096;
      gemv8_store(a, t, L, sb + (128 * g + 8 * l15) * 4, 1024, g < 2);
      asm volatile("s_waitcnt lgkmcnt(0)" ::: "memory");
#pragma unroll
      for (int k = 0; k < 8; ++k) {
        const f32x2 v = *(LAS f32x2*)(L + sb + k * 512 + lane * 8);
        st_sc1(v4u{__float_as_uint(v.x), ep, __float_as_uint(v.y), ep}, rs,
               WS_H3Y + ((fi * 4 + (k >> 1)) * 512 + 256 * wd2 + 128 * (k & 1) + 2 * lane) * 8);
      }
    }
    STAMP(7);
    const int aj = xcd * 16 + j, ob = aj >> 5, om0 = (aj & 31) * 16;
    float rsel = 0.f, mbo = 0.f, deno = 0.f;
    unsigned xv = 0;
    if (w == 0) {
      v4u wrv[4], xs[4];
#pragma unroll
      for (int k = 0; k < 4; ++k) xs[k] = ldx(x + g * 512 + l15 * 32 + k * 8);
#pragma unroll
      for (int k = 0; k < 4; ++k) wrv[k] = ldx(wr + l15 * 32 + k * 8);
      xv = *(const GAS unsigned*)(x + ob * 512 + om0 + 2 * (lane & 7));
      const Ln ln = ln_stats(xs);
      mbo = sel4(ob, ln.mb0, ln.mb1, ln.mb2, ln.mb3);
      deno = sel4(ob, ln.d0, ln.d1, ln.d2, ln.d3);
      const float mb = sel4(g, ln.mb0, ln.mb1, ln.mb2, ln.mb3), den = sel4(g, ln.d0, ln.d1, ln.d2, ln.d3);
      float r = 0.f;
#pragma unroll
      for (int k = 0; k < 4; ++k)
#pragma unroll
        for (int e = 0; e < 4; ++e) { r += xn1(bf_lo(xs[k][e]), mb, den) * bf_lo(wrv[k][e]); r += xn1(bf_hi(xs[k][e]), mb, den) * bf_hi(wrv[k][e]); }
      r = rbf(row16_sum(r));
      r = rbf(1.f / (1.f + __expf(-r)));
      rsel = rdlane(r, 16 * ob);
    }
    {
      const int piece = tid >> 3, sub = tid & 7;
      const bool yv = piece < 48;
      const int o0s = WS_H3S + ((piece * 4 + ob) * 512 + om0 + 2 * sub) * 8;
      const int oy = WS_H3Y + ((piece * 4 + ob) * 512 + om0 + 2 * sub) * 8;
      const int oy2 = oy + (yv ? 64 * 4 * 512 * 8 : 0);
      for (unsigned spins = 0; spins <= SPIN_CAP; ++spins) {
        asm volatile("" ::: "memory");
        const v4u t1 = ld_sc1(rs, WS_HINT3 + (8 * w + (lane & 7) + 64 * ((lane >> 3) & 1)) * 128);
        if (__all((t1.x == ep) & (t1.z == ep))) break;
        __builtin_amdgcn_s_sleep(POLL_SLEEP);
      }
      STAMP(8);
      v4u a, c, y0, y1;
      for (unsigned spins = 0;; ++spins) {
        asm volatile("" ::: "memory");
        y0 = ld_sc1(rs, oy);
        y1 = ld_sc1(rs, oy2);
        if (__all((y0.y == ep) & (y0.w == ep) & (y1.y == ep) & (y1.w == ep)) || spins > SPIN_CAP) break;
        __builtin_amdgcn_s_sleep(POLL_SLEEP);
      }
      __builtin_amdgcn_s_sleep(H3_DELAY);
      for (unsigned spins = 0;; ++spins) {
        asm volatile("" ::: "memory");
        a = ld_sc1(rs, o0s);
        c = ld_sc1(rs, o0s + 64 * 4 * 512 * 8);
        if (__all((a.y == ep) & (a.w == ep) & (c.y == ep) & (c.w == ep)) || spins > SPIN_CAP) break;
        __builtin_amdgcn_s_sleep(2);
      }
      f32x4 v = {__uint_as_float(a.x) + __uint_as_float(c.x), __uint_as_float(a.z) + __uint_as_float(c.z),
                 __uint_as_float(y0.x) + (yv ? __uint_as_float(y1.x) : 0.f), __uint_as_float(y0.z) + (yv ? __uint_as_float(y1.z) : 0.f)};
#pragma unroll
      for (int e = 0; e < 4; ++e) v[e] = x32_sum(x16_sum(v[e] + dpp<0x128>(v[e])));
      if (lane < 8) lds_wf4(L, L_SR + (w * 8 + lane) * 16, v);
      BAR();
      STAMP(9);
      if (w == 0) {
        f32x4 t = {0.f, 0.f, 0.f, 0.f};
#pragma unroll
        for (int k = 0; k < 8; ++k) t += lds_rf4(L, L_SR + (k * 8 + (lane & 7)) * 16);
        if (lane < 8) {
          const float mb = mbo, den = deno;
          float o2[2];
#pragma unroll
          for (int e = 0; e < 2; ++e) {
            const float xx = e ? bf_hi(xv) : bf_lo(xv);
            const float yb = rbf(rbf(t[2 + e]) * rsel);
            const float z = rbf(xn1(xx, mb, den) + yb);
            const float gg = rbf(silu_f(z));
            o2[e] = xx + rbf(rbf(t[e]) * gg);
          }
          *(GAS unsigned*)(out + ob * 512 + om0 + 2 * lane) = pk_bf16(o2[0], o2[1]);
        }
        if (aj == 0 && lane == 0) __hip_atomic_store((GAS unsigned*)(ws + WS_EPOCH), ep, RLX_AGENT);
      }
    }
    STAMP(13);
    DUMP();
  }
}

extern "C" void kernel_launch(void* const* d_in, const int* in_sizes, int n_in, void* d_out, int out_size, void* d_ws, size_t ws_size,
                              hipStream_t stream) {
  (void)hipFuncSetAttribute((const void*)mla_moe_kernel, hipFuncAttributeMaxDynamicSharedMemorySize, L_TOTAL);
  mla_moe_kernel<<<256, 512, L_TOTAL, stream>>>((const u16*)d_in[0], (const u16*)d_in[1], (const u16*)d_in[2], (const u16*)d_in[3],
                                                (const u16*)d_in[4], (const u16*)d_in[5], (const u16*)d_in[7], (const u16*)d_in[8],
                                                (const u16*)d_in[9], (const u16*)d_in[10], (u16*)d_out, (char*)d_ws);
}
```

```cpp
#define STAMPS
#include <cstdio>
#include <hip/hip_runtime.h>
#include <hip/hip_bf16.h>
#include <stdint.h>

typedef unsigned short u16;
typedef unsigned v4u __attribute__((ext_vector_type(4)));
typedef unsigned v2u __attribute__((ext_vector_type(2)));
typedef float f32x2 __attribute__((ext_vector_type(2)));
typedef float f32x4 __attribute__((ext_vector_type(4)));
typedef short bf16x8 __attribute__((ext_vector_type(8)));
typedef __bf16 bf2 __attribute__((ext_vector_type(2)));
#define DI __device__ __forceinline__
#define GAS __attribute__((address_space(1)))
#define LAS __attribute__((address_space(3)))
#define RLX_AGENT __ATOMIC_RELAXED, __HIP_MEMORY_SCOPE_AGENT

constexpr int WS_EPOCH = 0;
constexpr int WS_HINT1 = 16384;
constexpr int WS_HINT3 = 32768;
constexpr int WS_HINT2 = 49152;
constexpr int WS_H0 = 65536;
constexpr int WS_H1 = WS_H0 + 512 * 1024;
constexpr int WS_H2S = WS_H1 + 256 * 1024;
constexpr int WS_H3S = WS_H2S + 64 * 1024;
constexpr int WS_H3Y = WS_H3S + 2048 * 1024;
constexpr int WS_F1 = WS_H3Y + 2048 * 1024;
constexpr int WS_H2P = WS_F1 + 1024 * 1024;
constexpr int WS_TOTAL = WS_H2P + 4096 * 1024;
constexpr int NSTAMP = 16;
constexpr size_t WS_STAMP = 64u << 20;
#define STAMPW(k, wv) do { stv[k] = __builtin_amdgcn_s_memrealtime(); } while (0)
#define STAMP(k) STAMPW(k, 0)
constexpr unsigned SPIN_CAP = 1u << 17;
constexpr unsigned SENT = 0xFFFFFFFFu;
#define H2_DELAY 24
#define H1_DELAY 4
#define W_UK_DELAY 4
#define H0_DELAY 8
#define A_FIRST 4
#define A_SECOND 200
#define H2_WAIT 280
#define NEARLY 2
#define H0_GATE 150
#define H1_GATE 350
#define A_DELAY 0
#define H3_DELAY 16
#define POLL_SLEEP 4

constexpr int L_QA = 131072;
constexpr int L_FR = L_QA;
constexpr int L_CNT = L_QA + 8192;
constexpr int L_MX = 0;
constexpr int L_LS = 1024;
constexpr int L_P = 2048;
constexpr int L_SR = 16384;
constexpr int L_HM = 17408;
constexpr int L_FS = 20480;
constexpr int L_OST = 32768;
constexpr int L_TOTAL = L_QA + 32768;

DI float bf_lo(unsigned u) { return __uint_as_float(u << 16); }
DI float bf_hi(unsigned u) { return __uint_as_float(u & 0xFFFF0000u); }
DI unsigned pk_bf16(float lo, float hi) { f32x2 v = {lo, hi}; return __builtin_bit_cast(unsigned, __builtin_convertvector(v, bf2)); }
DI float rbf(float f) { return bf_lo(pk_bf16(f, f)); }
DI f32x4 mfma16(v4u a, v4u b, f32x4 c) { return __builtin_amdgcn_mfma_f32_16x16x32_bf16(__builtin_bit_cast(bf16x8, a), __builtin_bit_cast(bf16x8, b), c, 0, 0, 0); }
template <int CTRL> DI float dpp(float v) { return __builtin_bit_cast(float, __builtin_amdgcn_update_dpp(0, __builtin_bit_cast(int, v), CTRL, 0xF, 0xF, true)); }
DI float row16_sum(float v) { v += dpp<0xB1>(v); v += dpp<0x4E>(v); v += dpp<0x141>(v); v += dpp<0x140>(v); return v; }
DI float row16_max(float v) { v = fmaxf(v, dpp<0xB1>(v)); v = fmaxf(v, dpp<0x4E>(v)); v = fmaxf(v, dpp<0x141>(v)); v = fmaxf(v, dpp<0x140>(v)); return v; }
DI float rdlane(float v, int l) { return __builtin_bit_cast(float, __builtin_amdgcn_readlane(__builtin_bit_cast(int, v), l)); }
DI void x32_both(float v, float& lo, float& hi) {
  unsigned a = __builtin_bit_cast(unsigned, v), b = a;
  asm volatile("" : "+v"(b));
  auto r = __builtin_amdgcn_permlane32_swap(a, b, false, false);
  lo = __builtin_bit_cast(float, (unsigned)r[0]); hi = __builtin_bit_cast(float, (unsigned)r[1]);
}
DI void x16_both(float v, float& lo, float& hi) {
  unsigned a = __builtin_bit_cast(unsigned, v), b = a;
  asm volatile("" : "+v"(b));
  auto r = __builtin_amdgcn_permlane16_swap(a, b, false, false);
  lo = __builtin_bit_cast(float, (unsigned)r[0]); hi = __builtin_bit_cast(float, (unsigned)r[1]);
}
DI float x32_sum(float v) { float a, b; x32_both(v, a, b); return a + b; }
DI float x32_max(float v) { float a, b; x32_both(v, a, b); return fmaxf(a, b); }
DI float x16_sum(float v) { float a, b; x16_both(v, a, b); return a + b; }
DI float x16_max(float v) { float a, b; x16_both(v, a, b); return fmaxf(a, b); }
DI v4u ldg(const u16* p) { return __builtin_nontemporal_load((const GAS v4u*)p); }
DI v4u ldx(const u16* p) { return *(const GAS v4u*)p; }
DI v4u ld_sc1(__amdgpu_buffer_rsrc_t rs, int off) { return __builtin_amdgcn_raw_buffer_load_b128(rs, off, 0, 16); }
DI void st_sc1(v4u v, __amdgpu_buffer_rsrc_t rs, int off) { __builtin_amdgcn_raw_buffer_store_b128(v, rs, off, 0, 16); }
#define BAR() do { asm volatile("s_waitcnt lgkmcnt(0)" ::: "memory"); __builtin_amdgcn_s_barrier(); asm volatile("" ::: "memory"); } while (0)
#define PIN() do { asm volatile("" ::: "memory"); __builtin_amdgcn_sched_barrier(0); } while (0)
#define VM0() asm volatile("s_waitcnt vmcnt(0)" ::: "memory")
DI v4u lds_r128(LAS char* L, int off) { return *(LAS v4u*)(L + off); }
DI f32x4 lds_rf4(LAS char* L, int off) { return *(LAS f32x4*)(L + off); }
DI void lds_wf4(LAS char* L, int off, f32x4 v) { *(LAS f32x4*)(L + off) = v; }

DI void transpose8(const v4u (&r)[8], v4u (&t)[8]) {
#pragma unroll
  for (int i = 0; i < 8; ++i)
#pragma unroll
    for (int p = 0; p < 4; ++p)
      t[i][p] = __builtin_amdgcn_perm(r[2 * p + 1][i >> 1], r[2 * p][i >> 1], (i & 1) ? 0x07060302u : 0x05040100u);
}
DI void gemv8_store(v4u a, const v4u (&t)[8], LAS char* L, int off, int bstride, bool valid) {
  f32x4 d[8];
#pragma unroll
  for (int i = 0; i < 8; ++i) d[i] = mfma16(a, t[i], f32x4{0.f, 0.f, 0.f, 0.f});
  if (valid) {
#pragma unroll
    for (int bb = 0; bb < 4; ++bb) {
      lds_wf4(L, off + bb * bstride, f32x4{d[0][bb], d[1][bb], d[2][bb], d[3][bb]});
      lds_wf4(L, off + bb * bstride + 16, f32x4{d[4][bb], d[5][bb], d[6][bb], d[7][bb]});
    }
  }
}

struct Ln { float mb0, mb1, mb2, mb3, d0, d1, d2, d3; };
DI Ln ln_stats(const v4u (&xs)[4]) {
  float s = 0.f;
#pragma unroll
  for (int k = 0; k < 4; ++k)
#pragma unroll
    for (int e = 0; e < 4; ++e) s += bf_lo(xs[k][e]) + bf_hi(xs[k][e]);
  const float mean = row16_sum(s) * (1.f / 512.f);
  float q = 0.f;
#pragma unroll
  for (int k = 0; k < 4; ++k)
#pragma unroll
    for (int e = 0; e < 4; ++e) { const float a = bf_lo(xs[k][e]) - mean, b = bf_hi(xs[k][e]) - mean; q += a * a; q += b * b; }
  const float var = rbf(row16_sum(q) * (1.f / 512.f));
  const float mb = rbf(mean), den = 1.f / rbf(sqrtf(rbf(var + 1e-5f)));
  Ln o;
  o.mb0 = rdlane(mb, 0); o.mb1 = rdlane(mb, 16); o.mb2 = rdlane(mb, 32); o.mb3 = rdlane(mb, 48);
  o.d0 = rdlane(den, 0); o.d1 = rdlane(den, 16); o.d2 = rdlane(den, 32); o.d3 = rdlane(den, 48);
  return o;
}
DI float sel4(int b, float a0, float a1, float a2, float a3) { const float lo = (b & 1) ? a1 : a0, hi = (b & 1) ? a3 : a2; return (b & 2) ? hi : lo; }
DI float xn1(float x, float mb, float rden) { return rbf(rbf(x - mb) * rden); }
DI v4u xn_frag(v4u x, float mb, float rden) {
  v4u o;
#pragma unroll
  for (int e = 0; e < 4; ++e) o[e] = pk_bf16(rbf(bf_lo(x[e]) - mb) * rden, rbf(bf_hi(x[e]) - mb) * rden);
  return o;
}
DI unsigned swz(unsigned row) { return ((row & 3) << 2) | ((row >> 2) & 3); }
DI float silu_f(float z) { return z / (1.f + __expf(-z)); }

#define LCNT_ADD(o) do { asm volatile("s_waitcnt lgkmcnt(0)" ::: "memory"); if (lane == 0) __hip_atomic_fetch_add((LAS unsigned*)(L + (o)), 1u, __ATOMIC_RELAXED, __HIP_MEMORY_SCOPE_WORKGROUP); } while (0)
#define LCNT_WAIT(o, n) do { while (__builtin_amdgcn_readfirstlane(__hip_atomic_load((LAS unsigned*)(L + (o)), __ATOMIC_RELAXED, __HIP_MEMORY_SCOPE_WORKGROUP)) < (n)) {} asm volatile("" ::: "memory"); } while (0)

#define DUMP() do { if (tid == 0 && (blockIdx.x & 127) < 8) printf("blk %d abs0 %llu: %d %d %d %d %d %d %d %d %d %d %d %d %d %d %d\n", blockIdx.x, stv[0] % 100000, (int)(stv[1]-stv[0]), (int)(stv[2]-stv[0]), (int)(stv[3]-stv[0]), (int)(stv[4]-stv[0]), (int)(stv[5]-stv[0]), (int)(stv[6]-stv[0]), (int)(stv[7]-stv[0]), (int)(stv[8]-stv[0]), (int)(stv[9]-stv[0]), (int)(stv[10]-stv[0]), (int)(stv[11]-stv[0]), (int)(stv[12]-stv[0]), (int)(stv[13]-stv[0]), (int)(stv[14]-stv[0]), (int)(stv[15]-stv[0])); } while (0)
__global__ __launch_bounds__(512, 2) void mla_moe_kernel(const u16* x, const u16* ckv, const u16* wq, const u16* wuk, const u16* wuv,
    const u16* wo, const u16* wr, const u16* wg, const u16* wu, const u16* wd, u16* out, char* ws) {
  extern __shared__ __attribute__((aligned(16))) char smem[];
  LAS char* L = (LAS char*)smem;
  {
    unsigned long long pc0;
    asm volatile("s_getpc_b64 %0" : "=s"(pc0));
    const unsigned voff = threadIdx.x * 16;
    const unsigned ldst = (unsigned)(size_t)L + 131072 + 8192;
    unsigned keep;
    asm volatile("s_mov_b32 %0, m0\n\ts_mov_b32 m0, %3\n\ts_nop 0\n\t"
                 "global_load_lds_dwordx4 %1, %2\n\t"
                 "global_load_lds_dwordx4 %1, %4\n\t"
                 "global_load_lds_dwordx4 %1, %5\n\t"
                 "s_mov_b32 m0, %0"
                 : "=&s"(keep) : "v"(voff), "s"(pc0 & ~255ull), "s"(ldst), "s"((pc0 & ~255ull) + 8192), "s"((pc0 & ~255ull) + 16384) : "memory");
  }
  const int tid = threadIdx.x, lane = tid & 63, w = __builtin_amdgcn_readfirstlane(tid >> 6);
  const int xcd = blockIdx.x & 7, idx = blockIdx.x >> 3;
  const bool roleW = idx < 16;
  const int j = idx & 15;
  const int g = lane >> 4, l15 = lane & 15;
  unsigned long long stv[NSTAMP];
#pragma unroll
  for (int i = 0; i < NSTAMP; ++i) stv[i] = 0;
  STAMP(0);
  const unsigned long long t_start = __builtin_amdgcn_s_memrealtime();
  const __amdgpu_buffer_rsrc_t rs = __builtin_amdgcn_make_buffer_rsrc(ws, 0, WS_TOTAL, 0x00020000);

  if (__builtin_expect(roleW, 1)) {
    constexpr int L_RED = 0, L_QS = 8192, L_Q2 = 9216, L_CNTW = 10240, L_CCB = 12288, L_RED2 = 17408, L_CX = 26624, L_STG = 36864, L_FRW = 53248, L_WK = 65536;
    const int h = xcd * 4 + (j >> 2), j4 = j & 3, ch = j4 >> 1, dh = j4 & 1, pw = xcd * 16 + j;
    const int fgrp = xcd * 2 + (j >> 3), fkq = j & 7, fi = fgrp * 8 + fkq;
    const bool ffn = fgrp < 14;
    const unsigned ep_v = __hip_atomic_load((GAS unsigned*)(ws + WS_EPOCH), RLX_AGENT);
    unsigned ep;
    const int ks2 = (lane >> 3) & 1, nb8 = lane & 7;
    v4u wa[8], wb[8];
#define LOAD_WO() _Pragma("unroll") for (int r = 0; r < 8; ++r) wb[r] = ldg(wo + (size_t)(h * 128 + 64 * dh + 32 * ks2 + 8 * g + r) * 512 + 64 * w + 8 * nb8)
    if (__builtin_expect(w >= 4, 1)) {
      const int m0 = 128 * j4 + 32 * (w - 4);
      v4u wt[8], xs[4];
#pragma unroll
      for (int k = 0; k < 4; ++k) xs[k] = ldx(x + g * 512 + l15 * 32 + k * 8);
      const v4u xf = ldx(x + (lane & 3) * 512 + m0 + 8 * g);
      const int k0 = 64 * fkq + 32 * (w & 1);
      const v4u fx = ldx(x + (lane & 3) * 512 + k0 + 8 * g);
#pragma unroll
      for (int r = 0; r < 8; ++r) wt[r] = ldg(wq + (size_t)(m0 + 8 * g + r) * 4096 + h * 128 + 8 * l15);
      PIN();
      STAMP(3);
      const Ln ln = ln_stats(xs);
      ep = __builtin_amdgcn_readfirstlane(ep_v) + 1u;
      const int rb = lane & 3;
      const float mb = sel4(rb, ln.mb0, ln.mb1, ln.mb2, ln.mb3), den = sel4(rb, ln.d0, ln.d1, ln.d2, ln.d3);
      const v4u a = xn_frag(xf, mb, den), a0 = xn_frag(fx, mb, den);
      PIN();
      STAMP(5);
      v4u t[8];
      transpose8(wt, t);
      gemv8_store(a, t, L, L_RED + ((w - 4) * 4 * 128 + 8 * lane) * 4, 512, lane < 16);
      STAMP(11);
      BAR();
      BAR();
      BAR();
      if (ffn) {
        const u16* wsrc = (w < 6 ? wg : wu) + (size_t)(k0 + 8 * g) * 1792 + 128 * fgrp + 8 * l15;
        v4u wf[8];
#pragma unroll
        for (int r = 0; r < 8; ++r) wf[r] = ldg(wsrc + (size_t)r * 1792);
        PIN();
        LOAD_WO();
        PIN();
        transpose8(wf, t);
        gemv8_store(a0, t, L, L_FRW + ((w - 4) * 512 + 8 * lane) * 4, 512, lane < 16);
        LCNT_ADD(L_CNTW);
        LCNT_WAIT(L_CNTW, 4u);
#pragma unroll
        for (int k = 0; k < 2; ++k) {
          const int mat = (w - 4) >> 1, bb = 2 * (w & 1) + k;
          const f32x2 v = *(LAS f32x2*)(L + L_FRW + ((2 * mat) * 512 + bb * 128 + 2 * lane) * 4) + *(LAS f32x2*)(L + L_FRW + ((2 * mat + 1) * 512 + bb * 128 + 2 * lane) * 4);
          st_sc1(v4u{__float_as_uint(v.x), ep, __float_as_uint(v.y), ep}, rs, WS_F1 + (((fi * 2 + mat) * 4 + bb) * 128 + 2 * lane) * 8);
        }
        PIN();
      } else {
        LOAD_WO();
        PIN();
      }
    } else {
      const int c0 = 128 * j4 + 32 * w;
      if (tid == 0) *(LAS v2u*)(L + L_CNTW) = v2u{0u, 0u};
      ep = __builtin_amdgcn_readfirstlane(ep_v) + 1u;
      PIN();
#pragma unroll
      for (int i = 0; i < 8; ++i) {
        const int row = 4 * i + g;
        __builtin_amdgcn_global_load_lds((const GAS unsigned*)(wuk + (size_t)(c0 + row) * 4096 + h * 128 + (l15 ^ (row & 15)) * 8),
                                         (LAS unsigned*)(L + L_WK + w * 8192 + i * 1024), 16, 0, 2);
      }
      PIN();
      BAR();
      STAMP(1);
      {
        f32x2 s = {0.f, 0.f};
#pragma unroll
        for (int k = 0; k < 4; ++k) s += *(LAS f32x2*)(L + L_RED + ((k * 4 + w) * 128 + 2 * lane) * 4);
        st_sc1(v4u{__float_as_uint(s.x), ep, __float_as_uint(s.y), ep}, rs, WS_H0 + (((h * 4 + j4) * 4 + w) * 64 + lane) * 16);
      }
      PIN();
      BAR();
      asm volatile("s_waitcnt vmcnt(1)" ::: "memory");
      v4u wk[8];
#pragma unroll
      for (int rt = 0; rt < 2; ++rt)
#pragma unroll
        for (int ks = 0; ks < 4; ++ks) wk[rt * 4 + ks] = lds_r128(L, L_WK + w * 8192 + (16 * rt + l15) * 256 + 16 * ((4 * ks + g) ^ l15));
      __builtin_amdgcn_s_sleep(H0_DELAY);
      v4u v[4];
      for (unsigned spins = 0;; ++spins) {
        asm volatile("" ::: "memory");
        bool ok = true;
#pragma unroll
        for (int mq = 0; mq < 4; ++mq) v[mq] = ld_sc1(rs, WS_H0 + (((h * 4 + mq) * 4 + w) * 64 + lane) * 16);
#pragma unroll
        for (int mq = 0; mq < 4; ++mq) ok &= (v[mq].y == ep) & (v[mq].w == ep);
        if (__all(ok) || spins > SPIN_CAP) break;
        __builtin_amdgcn_s_sleep(2);
      }
      if (tid == 0) st_sc1(v4u{ep, ep, ep, ep}, rs, WS_HINT1 + pw * 128);
      STAMP(12);
      const float q0 = (__uint_as_float(v[0].x) + __uint_as_float(v[1].x)) + (__uint_as_float(v[2].x) + __uint_as_float(v[3].x));
      const float q1 = (__uint_as_float(v[0].z) + __uint_as_float(v[1].z)) + (__uint_as_float(v[2].z) + __uint_as_float(v[3].z));
      *(LAS unsigned*)(L + L_QS + (w * 128 + 2 * lane) * 2) = pk_bf16(q0, q1);
      LCNT_ADD(L_CNTW + 4);
      LCNT_WAIT(L_CNTW + 4, 4u);
      v4u bq[4];
#pragma unroll
      for (int ks = 0; ks < 4; ++ks) bq[ks] = lds_r128(L, L_QS + ((lane & 3) * 128 + 32 * ks + 8 * g) * 2);
#pragma unroll
      for (int rt = 0; rt < 2; ++rt) {
        f32x4 d = {0.f, 0.f, 0.f, 0.f};
#pragma unroll
        for (int ks = 0; ks < 4; ++ks) d = mfma16(wk[rt * 4 + ks], bq[ks], d);
        if (l15 < 4) *(LAS v2u*)(L + L_Q2 + w * 256 + l15 * 64 + (4 * rt + g) * 8) = v2u{pk_bf16(d[0], d[1]), pk_bf16(d[2], d[3])};
      }
      asm volatile("s_waitcnt lgkmcnt(0)" ::: "memory");
      if (lane < 32) {
        const v2u d = *(LAS v2u*)(L + L_Q2 + w * 256 + lane * 8);
        st_sc1(v4u{d.x, ep, d.y, ep}, rs, WS_H1 + (((lane >> 3) * 32 + h) * 128 + (c0 >> 2) + (lane & 7)) * 16);
      }
      STAMP(14);
      PIN();
      BAR();
      __builtin_amdgcn_s_sleep(2);
#pragma unroll
      for (int r = 0; r < 8; ++r) wa[r] = ldg(wuv + (size_t)(256 * ch + 64 * w + 32 * ks2 + 8 * g + r) * 4096 + h * 128 + 64 * dh + 8 * nb8);
      LOAD_WO();
      PIN();
    }
    STAMP(6);
    v4u wvT[8], woT[8];
    {
      if (w < 4) transpose8(wa, wvT);
      transpose8(wb, woT);
#pragma unroll
      for (int i = 0; i < 8; ++i) asm volatile("" : "+v"(woT[i]), "+v"(wvT[i]));
    }
    STAMP(7);
    const int bb = w >> 1;
    {
      {
        v4u t1;
        for (unsigned spins = 0; spins <= SPIN_CAP; ++spins) {
          asm volatile("" ::: "memory");
          t1 = ld_sc1(rs, WS_HINT2 + (bb * 32 + (lane & 31)) * 128);
          if (__all((t1.x == ep) & (t1.z == ep))) break;
          __builtin_amdgcn_s_sleep(POLL_SLEEP);
        }
        while (__any((unsigned)__builtin_amdgcn_s_memrealtime() - t1.y < H2_WAIT)) __builtin_amdgcn_s_sleep(1);
      }
      STAMP(8);
      v4u p[8], s;
      const int base = WS_H2P + (((bb * 32 + g) * 32 + h) * 512 + 256 * ch + 128 * (w & 1) + 8 * l15) * 2;
      for (unsigned spins = 0;; ++spins) {
        asm volatile("" ::: "memory");
        s = ld_sc1(rs, WS_H2S + ((bb * 32 + (lane & 31)) * 32 + h) * 16);
#pragma unroll
        for (int i = 0; i < 8; ++i) p[i] = ld_sc1(rs, base + (i ^ (4 * dh)) * (4 * 32 * 512 * 2));
        bool ok = (s.y == ep) & (s.w == ep);
#pragma unroll
        for (int i = 0; i < 8; ++i) ok &= (p[i].x != SENT) & (p[i].y != SENT) & (p[i].z != SENT) & (p[i].w != SENT);
        const bool allok = __all(ok);
        if (spins == 0) STAMP(2);
        if (allok || spins > SPIN_CAP) break;
        __builtin_amdgcn_s_sleep(2);
      }
      float wgt;
      {
        const float m = __uint_as_float(s.x), l = __uint_as_float(s.z);
        const float Mx = x16_max(row16_max(m));
        const float e = __builtin_amdgcn_exp2f(m - Mx);
        wgt = e * __builtin_amdgcn_rcpf(x16_sum(row16_sum(e * l)));
      }
      if (tid == 0) st_sc1(v4u{ep, ep, ep, ep}, rs, WS_HINT3 + pw * 128);
      STAMP(15);
      float acc[8] = {0.f, 0.f, 0.f, 0.f, 0.f, 0.f, 0.f, 0.f};
#pragma unroll
      for (int i = 0; i < 8; ++i) {
        const int s0 = 4 * (i ^ (4 * dh));
        const float ww = sel4(g, rdlane(wgt, s0), rdlane(wgt, s0 + 1), rdlane(wgt, s0 + 2), rdlane(wgt, s0 + 3));
#pragma unroll
        for (int e = 0; e < 4; ++e) { acc[2 * e] += ww * bf_lo(p[i][e]); acc[2 * e + 1] += ww * bf_hi(p[i][e]); }
      }
#pragma unroll
      for (int e = 0; e < 8; ++e) acc[e] = x32_sum(x16_sum(acc[e]));
      if (lane < 16)
        *(LAS v4u*)(L + L_CCB + bb * 576 + (128 * (w & 1) + 8 * lane) * 2) =
            v4u{pk_bf16(acc[0], acc[1]), pk_bf16(acc[2], acc[3]), pk_bf16(acc[4], acc[5]), pk_bf16(acc[6], acc[7])};
    }
    BAR();
    STAMP(9);
    const int rb = lane & 3, rks = (lane >> 2) & 1;
    const bool dvalid = g == ks2;
    if (w < 4) {
      const v4u a = lds_r128(L, L_CCB + rb * 576 + (64 * w + 32 * rks + 8 * g) * 2);
      gemv8_store(a, wvT, L, L_RED2 + ((w * 2 + ks2) * 4 * 64 + 8 * nb8) * 4, 256, dvalid);
    }
    BAR();
    {
      f32x4 a0 = {0.f, 0.f, 0.f, 0.f};
#pragma unroll
      for (int p = 0; p < 8; ++p) a0 += lds_rf4(L, L_RED2 + p * 1024 + lane * 16);
      *(LAS v2u*)(L + L_CX + w * 512 + lane * 8) = v2u{pk_bf16(a0[0], a0[1]), pk_bf16(a0[2], a0[3])};
      asm volatile("s_waitcnt lgkmcnt(0)" ::: "memory");
      const v4u a = lds_r128(L, L_CX + w * 512 + rb * 128 + (32 * rks + 8 * g) * 2);
      gemv8_store(a, woT, L, L_STG + ((w * 2 + ks2) * 4 * 64 + 8 * nb8) * 4, 256, dvalid);
      asm volatile("s_waitcnt lgkmcnt(0)" ::: "memory");
#pragma unroll
      for (int k = 0; k < 2; ++k) {
        const f32x2 v = *(LAS f32x2*)(L + L_STG + w * 2048 + k * 512 + lane * 8) + *(LAS f32x2*)(L + L_STG + w * 2048 + 1024 + k * 512 + lane * 8);
        st_sc1(v4u{__float_as_uint(v.x), ep, __float_as_uint(v.y), ep}, rs, WS_H3S + ((pw * 4 + 2 * k + (lane >> 5)) * 512 + 64 * w + 2 * (lane & 31)) * 8);
      }
    }
    STAMP(13);
      DUMP();
    return;
  }
  {
    const int b = xcd >> 1, slice = (xcd & 1) * 16 + j;
    const int fgrp = xcd * 2 + (j >> 3), fkq = j & 7, fi = fgrp * 8 + fkq;
    const bool ffn = fgrp < 14;
    unsigned ep_v;
    asm volatile("global_load_dword %0, %1, off sc1" : "=v"(ep_v) : "v"(ws + WS_EPOCH) : "memory");
    while (__builtin_amdgcn_s_memrealtime() - t_start < A_DELAY) __builtin_amdgcn_s_sleep(1);
    PIN();
    {
      const u16* ksrc = ckv + ((size_t)(b * 4096 + slice * 128 + 16 * w)) * 512 + g * 128;
#pragma unroll
      for (int i = 0; i < 16; ++i) {
        if (i == A_FIRST) { PIN(); while (__builtin_amdgcn_s_memrealtime() - t_start < A_SECOND) __builtin_amdgcn_s_sleep(1); PIN(); }
        __builtin_amdgcn_global_load_lds((const GAS unsigned*)(ksrc + (size_t)i * 512 + (l15 ^ swz(i)) * 8),
                                         (LAS unsigned*)(L + (16 * w + i) * 1024), 16, 0, 2);
      }
    }
    PIN();
    __builtin_amdgcn_s_barrier();
    const int pofs = WS_H2P + (((b * 32 + slice) * 32 + (lane >> 3)) * 512 + 64 * w + 8 * (lane & 7)) * 2;
#pragma unroll
    for (int i = 0; i < 4; ++i) st_sc1(v4u{SENT, SENT, SENT, SENT}, rs, pofs + i * 8192);
    PIN();
    asm volatile("s_waitcnt vmcnt(4)" : "+v"(ep_v) :: "memory");
    const unsigned ep = __builtin_amdgcn_readfirstlane(ep_v) + 1u;
    v4u ka[16];
    v2u tr[4][4][2];
    const unsigned lb = (unsigned)(size_t)L;
    {
      const int krow = 8 * ((l15 >> 2) & 1) + 4 * (l15 >> 3) + (l15 & 3);
      const int sk = swz(krow);
#pragma unroll
      for (int k4 = 0; k4 < 4; ++k4) {
        const unsigned addr = lb + (16 * w + krow) * 1024 + 16 * ((4 * k4 + g) ^ sk);
#pragma unroll
        for (int ct = 0; ct < 4; ++ct) asm volatile("ds_read_b128 %0, %1 offset:%2" : "=v"(ka[ct * 4 + k4]) : "v"(addr), "i"(ct * 256) : "memory");
      }
    }
    BAR();
    STAMP(10);
    {
      const int q4 = l15 >> 2, p4 = lane & 3;
      const unsigned row = 8 * (g & 1) + 4 * (g >> 1) + q4;
#pragma unroll
      for (int u = 0; u < 2; ++u) {
        const unsigned addr = lb + row * 1024 + (w >> 1) * 256 + 16 * ((8 * (w & 1) + 4 * u + p4) ^ swz(row));
#pragma unroll
        for (int s = 0; s < 4; ++s)
#pragma unroll
          for (int t2 = 0; t2 < 2; ++t2)
#pragma unroll
            for (int e = 0; e < 2; ++e)
              asm volatile("ds_read_b64_tr_b16 %0, %1 offset:%2" : "=v"(tr[s][u * 2 + e][t2]) : "v"(addr + (s >> 1) * 65536), "i"((s & 1) * 32768 + t2 * 16384 + 8 * e) : "memory");
      }
    }
    {
      const int qbase = WS_H1 + ((b * 32 + 4 * w) * 128 + lane) * 16;
      for (unsigned spins = 0; spins <= SPIN_CAP; ++spins) {
        asm volatile("" ::: "memory");
        const v4u t1 = ld_sc1(rs, WS_HINT1 + (16 * w + l15) * 128);
        if (__all((t1.x == ep) & (t1.z == ep))) { if (spins) __builtin_amdgcn_s_sleep(H1_DELAY); break; }
        __builtin_amdgcn_s_sleep(POLL_SLEEP);
      }
      STAMP(2);
      v4u qv[8];
      for (unsigned spins = 0;; ++spins) {
        asm volatile("" ::: "memory");
#pragma unroll
        for (int i = 0; i < 8; ++i) qv[i] = ld_sc1(rs, qbase + i * 1024);
        bool ok = true;
#pragma unroll
        for (int i = 0; i < 8; ++i) ok &= (qv[i].y == ep) & (qv[i].w == ep);
        if (__all(ok) || spins > SPIN_CAP) break;
        __builtin_amdgcn_s_sleep(2);
      }
      STAMP(11);
      const int gq = (lane >> 1) & 3;
#pragma unroll
      for (int i = 0; i < 8; ++i) {
        const int h16 = (4 * w + (i >> 1)) & 15, ks = 8 * (i & 1) + (lane >> 3);
        *(LAS v2u*)(L + L_QA + (w >> 2) * 16384 + ks * 1024 + ((h16 ^ (gq | ((ks & 1) << 2))) + 16 * gq) * 16 + 8 * (lane & 1)) = v2u{qv[i].x, qv[i].z};
      }
    }
    VM0();
    BAR();
#pragma unroll
    for (int s = 0; s < 4; ++s) {
      asm volatile("" : "+v"(tr[s][0][0]), "+v"(tr[s][0][1]), "+v"(tr[s][1][0]), "+v"(tr[s][1][1]), "+v"(tr[s][2][0]), "+v"(tr[s][2][1]), "+v"(tr[s][3][0]), "+v"(tr[s][3][1]));
      asm volatile("" : "+v"(ka[4 * s]), "+v"(ka[4 * s + 1]), "+v"(ka[4 * s + 2]), "+v"(ka[4 * s + 3]));
    }
    if (tid == 0) st_sc1(v4u{ep, (unsigned)__builtin_amdgcn_s_memrealtime(), ep, 0u}, rs, WS_HINT2 + (b * 32 + slice) * 128);
    STAMP(3);
    float sc[2][4];
    {
      constexpr int RING = 16;
      v4u bq[RING];
      const unsigned qa0 = (unsigned)(size_t)L + L_QA + ((l15 ^ g) + 16 * g) * 16, qa1 = (unsigned)(size_t)L + L_QA + ((l15 ^ (g | 4)) + 16 * g) * 16;
#define QREAD(i) asm volatile("ds_read_b128 %0, %1 offset:%2" : "=v"(bq[(i) % RING]) : "v"((((i) >> 1) & 1) ? qa1 : qa0), "i"(((i) & 1) * 16384 + ((i) >> 1) * 1024) : "memory")
#pragma unroll
      for (int i = 0; i < RING; ++i) QREAD(i);
      f32x4 acc[2] = {{0.f, 0.f, 0.f, 0.f}, {0.f, 0.f, 0.f, 0.f}};
#pragma unroll
      for (int i = 0; i < 32; i += 2) {
        asm volatile("s_waitcnt lgkmcnt(%2)" : "+v"(bq[i % RING]), "+v"(bq[(i + 1) % RING]) : "i"(i + RING <= 32 ? RING - 2 : 30 - i) : "memory");
        acc[0] = mfma16(ka[i >> 1], bq[i % RING], acc[0]);
        acc[1] = mfma16(ka[i >> 1], bq[(i + 1) % RING], acc[1]);
        __builtin_amdgcn_sched_barrier(0);
        if (i + RING < 32) { QREAD(i + RING); QREAD(i + RING + 1); }
      }
      constexpr float SC = 0.08838834764831845f * 1.4426950408889634f;
      float m[2];
#pragma unroll
      for (int hh = 0; hh < 2; ++hh) {
#pragma unroll
        for (int e = 0; e < 4; ++e) sc[hh][e] = acc[hh][e] * SC;
        m[hh] = fmaxf(fmaxf(sc[hh][0], sc[hh][1]), fmaxf(sc[hh][2], sc[hh][3]));
      }
      auto r1 = __builtin_amdgcn_permlane16_swap(__float_as_uint(m[0]), __float_as_uint(m[1]), false, false);
      const float mm = x32_max(fmaxf(__uint_as_float(r1[0]), __uint_as_float(r1[1])));
      if (lane < 32) *(LAS float*)(L + L_MX + (lane * 8 + w) * 4) = mm;
    }
    BAR();
    STAMP(4);
    float M[2];
    {
      f32x4 mx[2][2];
#pragma unroll
      for (int hh = 0; hh < 2; ++hh) { mx[hh][0] = lds_rf4(L, L_MX + (hh * 16 + l15) * 32); mx[hh][1] = lds_rf4(L, L_MX + (hh * 16 + l15) * 32 + 16); }
#pragma unroll
      for (int hh = 0; hh < 2; ++hh) {
        M[hh] = fmaxf(fmaxf(fmaxf(mx[hh][0][0], mx[hh][0][1]), fmaxf(mx[hh][0][2], mx[hh][0][3])), fmaxf(fmaxf(mx[hh][1][0], mx[hh][1][1]), fmaxf(mx[hh][1][2], mx[hh][1][3])));
        const v2u p = {pk_bf16(__builtin_amdgcn_exp2f(sc[hh][0] - M[hh]), __builtin_amdgcn_exp2f(sc[hh][1] - M[hh])),
                       pk_bf16(__builtin_amdgcn_exp2f(sc[hh][2] - M[hh]), __builtin_amdgcn_exp2f(sc[hh][3] - M[hh]))};
        *(LAS v2u*)(L + L_P + hh * 4096 + ((w >> 1) * 64 + lane) * 16 + 8 * (w & 1)) = p;
      }
    }
    BAR();
    STAMP(5);
    {
      v4u pb[2][4];
#pragma unroll
      for (int hh = 0; hh < 2; ++hh)
#pragma unroll
        for (int s = 0; s < 4; ++s) pb[hh][s] = lds_r128(L, L_P + hh * 4096 + (s * 64 + lane) * 16);
      if (w == 0) {
        const v4u ones = {0x3F803F80u, 0x3F803F80u, 0x3F803F80u, 0x3F803F80u};
        f32x4 l0 = {0.f, 0.f, 0.f, 0.f}, l1 = {0.f, 0.f, 0.f, 0.f};
#pragma unroll
        for (int s = 0; s < 4; ++s) { l0 = mfma16(ones, pb[0][s], l0); l1 = mfma16(ones, pb[1][s], l1); }
        if (lane < 32)
          st_sc1(v4u{__float_as_uint(lane < 16 ? M[0] : M[1]), ep, __float_as_uint(lane < 16 ? l0[0] : l1[0]), ep}, rs, WS_H2S + ((b * 32 + slice) * 32 + lane) * 16);
      }
#pragma unroll
      for (int hh = 0; hh < 2; ++hh) {
        f32x4 o[4];
#pragma unroll
        for (int c = 0; c < 4; ++c) o[c] = f32x4{0.f, 0.f, 0.f, 0.f};
#pragma unroll
        for (int s = 0; s < 4; ++s)
#pragma unroll
          for (int c = 0; c < 4; ++c) o[c] = mfma16(v4u{tr[s][c][0].x, tr[s][c][0].y, tr[s][c][1].x, tr[s][c][1].y}, pb[hh][s], o[c]);
#pragma unroll
        for (int u = 0; u < 2; ++u) {
          v4u v = {pk_bf16(o[2 * u][0], o[2 * u][1]), pk_bf16(o[2 * u][2], o[2 * u][3]), pk_bf16(o[2 * u + 1][0], o[2 * u + 1][1]), pk_bf16(o[2 * u + 1][2], o[2 * u + 1][3])};
#pragma unroll
          for (int e = 0; e < 4; ++e) v[e] = v[e] == SENT ? 0x7FC07FC0u : v[e];
          *(LAS v4u*)(L + L_OST + w * 4096 + (hh * 16 + l15) * 128 + 16 * ((4 * u + g) ^ (l15 & 7))) = v;
        }
#pragma unroll
        for (int i = 2 * hh; i < 2 * hh + 2; ++i) {
          const int hr = 8 * i + (lane >> 3);
          st_sc1(lds_r128(L, L_OST + w * 4096 + hr * 128 + 16 * ((lane & 7) ^ (hr & 7))), rs, pofs + i * 8192);
        }
      }
    }
    PIN();
    __builtin_amdgcn_s_barrier();
    STAMP(6);
    if (ffn && (w == 1 || w == 2)) {
      v4u wf[8];
      const int wd2 = w - 1;
#pragma unroll
      for (int r = 0; r < 8; ++r) wf[r] = ldg(wd + (size_t)(16 * fi + 8 * (g & 1) + r) * 512 + 256 * wd2 + 128 * (g >> 1) + 8 * l15);
      v4u gv[8];
      const int gofs = WS_F1 + ((fgrp * 8 * 8 + (lane >> 3)) * 128 + 16 * fkq + 2 * (lane & 7)) * 8;
      for (unsigned spins = 0;; ++spins) {
        asm volatile("" ::: "memory");
        bool ok = true;
#pragma unroll
        for (int k = 0; k < 8; ++k) gv[k] = ld_sc1(rs, gofs + k * 8192);
#pragma unroll
        for (int k = 0; k < 8; ++k) ok &= (gv[k].y == ep) & (gv[k].w == ep);
        if (__all(ok) || spins > SPIN_CAP) break;
        __builtin_amdgcn_s_sleep(8);
      }
      float s0 = 0.f, s1 = 0.f;
#pragma unroll
      for (int k = 0; k < 8; ++k) { s0 += __uint_as_float(gv[k].x); s1 += __uint_as_float(gv[k].z); }
      s0 = rbf(s0); s1 = rbf(s1);
      float g0, u0, g1, u1;
      x32_both(s0, g0, u0);
      x32_both(s1, g1, u1);
      if (lane < 32) *(LAS unsigned*)(L + L_HM + wd2 * 128 + lane * 4) = pk_bf16(rbf(silu_f(g0)) * u0, rbf(silu_f(g1)) * u1);
      asm volatile("s_waitcnt lgkmcnt(0)" ::: "memory");
      v4u a = lds_r128(L, L_HM + wd2 * 128 + ((lane & 3) * 16 + 8 * (g & 1)) * 2);
      if ((g >> 1) != ((lane >> 2) & 1)) a = v4u{0u, 0u, 0u, 0u};
      v4u t[8];
      transpose8(wf, t);
      const int sb = L_FS + wd2 * 4096;
      gemv8_store(a, t, L, sb + (128 * g + 8 * l15) * 4, 1024, g < 2);
      asm volatile("s_waitcnt lgkmcnt(0)" ::: "memory");
#pragma unroll
      for (int k = 0; k < 8; ++k) {
        const f32x2 v = *(LAS f32x2*)(L + sb + k * 512 + lane * 8);
        st_sc1(v4u{__float_as_uint(v.x), ep, __float_as_uint(v.y), ep}, rs,
               WS_H3Y + ((fi * 4 + (k >> 1)) * 512 + 256 * wd2 + 128 * (k & 1) + 2 * lane) * 8);
      }
    }
    STAMP(7);
    const int aj = xcd * 16 + j, ob = aj >> 5, om0 = (aj & 31) * 16;
    float rsel = 0.f, mbo = 0.f, deno = 0.f;
    unsigned xv = 0;
    if (w == 0) {
      v4u wrv[4], xs[4];
#pragma unroll
      for (int k = 0; k < 4; ++k) xs[k] = ldx(x + g * 512 + l15 * 32 + k * 8);
#pragma unroll
      for (int k = 0; k < 4; ++k) wrv[k] = ldx(wr + l15 * 32 + k * 8);
      xv = *(const GAS unsigned*)(x + ob * 512 + om0 + 2 * (lane & 7));
      const Ln ln = ln_stats(xs);
      mbo = sel4(ob, ln.mb0, ln.mb1, ln.mb2, ln.mb3);
      deno = sel4(ob, ln.d0, ln.d1, ln.d2, ln.d3);
      const float mb = sel4(g, ln.mb0, ln.mb1, ln.mb2, ln.mb3), den = sel4(g, ln.d0, ln.d1, ln.d2, ln.d3);
      float r = 0.f;
#pragma unroll
      for (int k = 0; k < 4; ++k)
#pragma unroll
        for (int e = 0; e < 4; ++e) { r += xn1(bf_lo(xs[k][e]), mb, den) * bf_lo(wrv[k][e]); r += xn1(bf_hi(xs[k][e]), mb, den) * bf_hi(wrv[k][e]); }
      r = rbf(row16_sum(r));
      r = rbf(1.f / (1.f + __expf(-r)));
      rsel = rdlane(r, 16 * ob);
    }
    {
      const int piece = tid >> 3, sub = tid & 7;
      const bool yv = piece < 48;
      const int o0s = WS_H3S + ((piece * 4 + ob) * 512 + om0 + 2 * sub) * 8;
      const int oy = WS_H3Y + ((piece * 4 + ob) * 512 + om0 + 2 * sub) * 8;
      const int oy2 = oy + (yv ? 64 * 4 * 512 * 8 : 0);
      for (unsigned spins = 0; spins <= SPIN_CAP; ++spins) {
        asm volatile("" ::: "memory");
        const v4u t1 = ld_sc1(rs, WS_HINT3 + (8 * w + (lane & 7) + 64 * ((lane >> 3) & 1)) * 128);
        if (__all((t1.x == ep) & (t1.z == ep))) break;
        __builtin_amdgcn_s_sleep(POLL_SLEEP);
      }
      STAMP(8);
      v4u a, c, y0, y1;
      for (unsigned spins = 0;; ++spins) {
        asm volatile("" ::: "memory");
        y0 = ld_sc1(rs, oy);
        y1 = ld_sc1(rs, oy2);
        if (__all((y0.y == ep) & (y0.w == ep) & (y1.y == ep) & (y1.w == ep)) || spins > SPIN_CAP) break;
        __builtin_amdgcn_s_sleep(POLL_SLEEP);
      }
      __builtin_amdgcn_s_sleep(H3_DELAY);
      for (unsigned spins = 0;; ++spins) {
        asm volatile("" ::: "memory");
        a = ld_sc1(rs, o0s);
        c = ld_sc1(rs, o0s + 64 * 4 * 512 * 8);
        if (__all((a.y == ep) & (a.w == ep) & (c.y == ep) & (c.w == ep)) || spins > SPIN_CAP) break;
        __builtin_amdgcn_s_sleep(2);
      }
      f32x4 v = {__uint_as_float(a.x) + __uint_as_float(c.x), __uint_as_float(a.z) + __uint_as_float(c.z),
                 __uint_as_float(y0.x) + (yv ? __uint_as_float(y1.x) : 0.f), __uint_as_float(y0.z) + (yv ? __uint_as_float(y1.z) : 0.f)};
#pragma unroll
      for (int e = 0; e < 4; ++e) v[e] = x32_sum(x16_sum(v[e] + dpp<0x128>(v[e])));
      if (lane < 8) lds_wf4(L, L_SR + (w * 8 + lane) * 16, v);
      BAR();
      STAMP(9);
      if (w == 0) {
        f32x4 t = {0.f, 0.f, 0.f, 0.f};
#pragma unroll
        for (int k = 0; k < 8; ++k) t += lds_rf4(L, L_SR + (k * 8 + (lane & 7)) * 16);
        if (lane < 8) {
          const float mb = mbo, den = deno;
          float o2[2];
#pragma unroll
          for (int e = 0; e < 2; ++e) {
            const float xx = e ? bf_hi(xv) : bf_lo(xv);
            const float yb = rbf(rbf(t[2 + e]) * rsel);
            const float z = rbf(xn1(xx, mb, den) + yb);
            const float gg = rbf(silu_f(z));
            o2[e] = xx + rbf(rbf(t[e]) * gg);
          }
          *(GAS unsigned*)(out + ob * 512 + om0 + 2 * lane) = pk_bf16(o2[0], o2[1]);
        }
        if (aj == 0 && lane == 0) __hip_atomic_store((GAS unsigned*)(ws + WS_EPOCH), ep, RLX_AGENT);
      }
    }
    STAMP(13);
    DUMP();
  }
}

extern "C" void kernel_launch(void* const* d_in, const int* in_sizes, int n_in, void* d_out, int out_size, void* d_ws, size_t ws_size,
                              hipStream_t stream) {
  (void)hipFuncSetAttribute((const void*)mla_moe_kernel, hipFuncAttributeMaxDynamicSharedMemorySize, L_TOTAL);
  mla_moe_kernel<<<256, 512, L_TOTAL, stream>>>((const u16*)d_in[0], (const u16*)d_in[1], (const u16*)d_in[2], (const u16*)d_in[3],
                                                (const u16*)d_in[4], (const u16*)d_in[5], (const u16*)d_in[7], (const u16*)d_in[8],
                                                (const u16*)d_in[9], (const u16*)d_in[10], (u16*)d_out, (char*)d_ws);
}
```

```cpp
#include <hip/hip_runtime.h>
#include <hip/hip_bf16.h>
#include <stdint.h>

typedef unsigned short u16;
typedef unsigned v4u __attribute__((ext_vector_type(4)));
typedef unsigned v2u __attribute__((ext_vector_type(2)));
typedef float f32x2 __attribute__((ext_vector_type(2)));
typedef float f32x4 __attribute__((ext_vector_type(4)));
typedef short bf16x8 __attribute__((ext_vector_type(8)));
typedef __bf16 bf2 __attribute__((ext_vector_type(2)));
#define DI __device__ __forceinline__
#define GAS __attribute__((address_space(1)))
#define LAS __attribute__((address_space(3)))
#define RLX_AGENT __ATOMIC_RELAXED, __HIP_MEMORY_SCOPE_AGENT

constexpr int WS_EPOCH = 0;
constexpr int WS_HINT1 = 16384;
constexpr int WS_HINT3 = 32768;
constexpr int WS_HINT2 = 49152;
constexpr int WS_H0 = 65536;
constexpr int WS_H1 = WS_H0 + 512 * 1024;
constexpr int WS_H2S = WS_H1 + 256 * 1024;
constexpr int WS_H3S = WS_H2S + 64 * 1024;
constexpr int WS_H3Y = WS_H3S + 2048 * 1024;
constexpr int WS_F1 = WS_H3Y + 2048 * 1024;
constexpr int WS_H2P = WS_F1 + 1024 * 1024;
constexpr int WS_TOTAL = WS_H2P + 4096 * 1024;
#define STAMPW(k, wv) do {} while (0)
#define STAMP(k) STAMPW(k, 0)
constexpr unsigned SPIN_CAP = 1u << 17;
constexpr unsigned SENT = 0xFFFFFFFFu;
#define H2_DELAY 24
#define H1_DELAY 4
#define W_UK_DELAY 4
#define H0_DELAY 8
#define A_FIRST 4
#define A_SECOND 200
#define H2_WAIT 280
#define NEARLY 2
#define H0_GATE 150
#define H1_GATE 350
#define A_DELAY 0
#define H3_DELAY 16
#define POLL_SLEEP 4

constexpr int L_QA = 131072;
constexpr int L_FR = L_QA;
constexpr int L_CNT = L_QA + 8192;
constexpr int L_MX = 0;
constexpr int L_LS = 1024;
constexpr int L_P = 2048;
constexpr int L_SR = 16384;
constexpr int L_HM = 17408;
constexpr int L_FS = 20480;
constexpr int L_OST = 32768;
constexpr int L_TOTAL = L_QA + 32768;

DI float bf_lo(unsigned u) { return __uint_as_float(u << 16); }
DI float bf_hi(unsigned u) { return __uint_as_float(u & 0xFFFF0000u); }
DI unsigned pk_bf16(float lo, float hi) { f32x2 v = {lo, hi}; return __builtin_bit_cast(unsigned, __builtin_convertvector(v, bf2)); }
DI float rbf(float f) { return bf_lo(pk_bf16(f, f)); }
DI f32x4 mfma16(v4u a, v4u b, f32x4 c) { return __builtin_amdgcn_mfma_f32_16x16x32_bf16(__builtin_bit_cast(bf16x8, a), __builtin_bit_cast(bf16x8, b), c, 0, 0, 0); }
template <int CTRL> DI float dpp(float v) { return __builtin_bit_cast(float, __builtin_amdgcn_update_dpp(0, __builtin_bit_cast(int, v), CTRL, 0xF, 0xF, true)); }
DI float row16_sum(float v) { v += dpp<0xB1>(v); v += dpp<0x4E>(v); v += dpp<0x141>(v); v += dpp<0x140>(v); return v; }
DI float row16_max(float v) { v = fmaxf(v, dpp<0xB1>(v)); v = fmaxf(v, dpp<0x4E>(v)); v = fmaxf(v, dpp<0x141>(v)); v = fmaxf(v, dpp<0x140>(v)); return v; }
DI float rdlane(float v, int l) { return __builtin_bit_cast(float, __builtin_amdgcn_readlane(__builtin_bit_cast(int, v), l)); }
DI void x32_both(float v, float& lo, float& hi) {
  unsigned a = __builtin_bit_cast(unsigned, v), b = a;
  asm volatile("" : "+v"(b));
  auto r = __builtin_amdgcn_permlane32_swap(a, b, false, false);
  lo = __builtin_bit_cast(float, (unsigned)r[0]); hi = __builtin_bit_cast(float, (unsigned)r[1]);
}
DI void x16_both(float v, float& lo, float& hi) {
  unsigned a = __builtin_bit_cast(unsigned, v), b = a;
  asm volatile("" : "+v"(b));
  auto r = __builtin_amdgcn_permlane16_swap(a, b, false, false);
  lo = __builtin_bit_cast(float, (unsigned)r[0]); hi = __builtin_bit_cast(float, (unsigned)r[1]);
}
DI float x32_sum(float v) { float a, b; x32_both(v, a, b); return a + b; }
DI float x32_max(float v) { float a, b; x32_both(v, a, b); return fmaxf(a, b); }
DI float x16_sum(float v) { float a, b; x16_both(v, a, b); return a + b; }
DI float x16_max(float v) { float a, b; x16_both(v, a, b); return fmaxf(a, b); }
DI v4u ldg(const u16* p) { return __builtin_nontemporal_load((const GAS v4u*)p); }
DI v4u ldx(const u16* p) { return *(const GAS v4u*)p; }
DI v4u ld_sc1(__amdgpu_buffer_rsrc_t rs, int off) { return __builtin_amdgcn_raw_buffer_load_b128(rs, off, 0, 16); }
DI void st_sc1(v4u v, __amdgpu_buffer_rsrc_t rs, int off) { __builtin_amdgcn_raw_buffer_store_b128(v, rs, off, 0, 16); }
#define BAR() do { asm volatile("s_waitcnt lgkmcnt(0)" ::: "memory"); __builtin_amdgcn_s_barrier(); asm volatile("" ::: "memory"); } while (0)
#define PIN() do { asm volatile("" ::: "memory"); __builtin_amdgcn_sched_barrier(0); } while (0)
#define VM0() asm volatile("s_waitcnt vmcnt(0)" ::: "memory")
DI v4u lds_r128(LAS char* L, int off) { return *(LAS v4u*)(L + off); }
DI f32x4 lds_rf4(LAS char* L, int off) { return *(LAS f32x4*)(L + off); }
DI void lds_wf4(LAS char* L, int off, f32x4 v) { *(LAS f32x4*)(L + off) = v; }

DI void transpose8(const v4u (&r)[8], v4u (&t)[8]) {
#pragma unroll
  for (int i = 0; i < 8; ++i)
#pragma unroll
    for (int p = 0; p < 4; ++p)
      t[i][p] = __builtin_amdgcn_perm(r[2 * p + 1][i >> 1], r[2 * p][i >> 1], (i & 1) ? 0x07060302u : 0x05040100u);
}
DI void gemv8_store(v4u a, const v4u (&t)[8], LAS char* L, int off, int bstride, bool valid) {
  f32x4 d[8];
#pragma unroll
  for (int i = 0; i < 8; ++i) d[i] = mfma16(a, t[i], f32x4{0.f, 0.f, 0.f, 0.f});
  if (valid) {
#pragma unroll
    for (int bb = 0; bb < 4; ++bb) {
      lds_wf4(L, off + bb * bstride, f32x4{d[0][bb], d[1][bb], d[2][bb], d[3][bb]});
      lds_wf4(L, off + bb * bstride + 16, f32x4{d[4][bb], d[5][bb], d[6][bb], d[7][bb]});
    }
  }
}

struct Ln { float mb0, mb1, mb2, mb3, d0, d1, d2, d3; };
DI Ln ln_stats(const v4u (&xs)[4]) {
  float s = 0.f;
#pragma unroll
  for (int k = 0; k < 4; ++k)
#pragma unroll
    for (int e = 0; e < 4; ++e) s += bf_lo(xs[k][e]) + bf_hi(xs[k][e]);
  const float mean = row16_sum(s) * (1.f / 512.f);
  float q = 0.f;
#pragma unroll
  for (int k = 0; k < 4; ++k)
#pragma unroll
    for (int e = 0; e < 4; ++e) { const float a = bf_lo(xs[k][e]) - mean, b = bf_hi(xs[k][e]) - mean; q += a * a; q += b * b; }
  const float var = rbf(row16_sum(q) * (1.f / 512.f));
  const float mb = rbf(mean), den = 1.f / rbf(sqrtf(rbf(var + 1e-5f)));
  Ln o;
  o.mb0 = rdlane(mb, 0); o.mb1 = rdlane(mb, 16); o.mb2 = rdlane(mb, 32); o.mb3 = rdlane(mb, 48);
  o.d0 = rdlane(den, 0); o.d1 = rdlane(den, 16); o.d2 = rdlane(den, 32); o.d3 = rdlane(den, 48);
  return o;
}
DI float sel4(int b, float a0, float a1, float a2, float a3) { const float lo = (b & 1) ? a1 : a0, hi = (b & 1) ? a3 : a2; return (b & 2) ? hi : lo; }
DI float xn1(float x, float mb, float rden) { return rbf(rbf(x - mb) * rden); }
DI v4u xn_frag(v4u x, float mb, float rden) {
  v4u o;
#pragma unroll
  for (int e = 0; e < 4; ++e) o[e] = pk_bf16(rbf(bf_lo(x[e]) - mb) * rden, rbf(bf_hi(x[e]) - mb) * rden);
  return o;
}
DI unsigned swz(unsigned row) { return ((row & 3) << 2) | ((row >> 2) & 3); }
DI float silu_f(float z) { return z / (1.f + __expf(-z)); }

#define LCNT_ADD(o) do { asm volatile("s_waitcnt lgkmcnt(0)" ::: "memory"); if (lane == 0) __hip_atomic_fetch_add((LAS unsigned*)(L + (o)), 1u, __ATOMIC_RELAXED, __HIP_MEMORY_SCOPE_WORKGROUP); } while (0)
#define LCNT_WAIT(o, n) do { while (__builtin_amdgcn_readfirstlane(__hip_atomic_load((LAS unsigned*)(L + (o)), __ATOMIC_RELAXED, __HIP_MEMORY_SCOPE_WORKGROUP)) < (n)) {} asm volatile("" ::: "memory"); } while (0)

#define DUMP() do {} while (0)
__global__ __launch_bounds__(512, 2) void mla_moe_kernel(const u16* x, const u16* ckv, const u16* wq, const u16* wuk, const u16* wuv,
    const u16* wo, const u16* wr, const u16* wg, const u16* wu, const u16* wd, u16* out, char* ws) {
  extern __shared__ __attribute__((aligned(16))) char smem[];
  LAS char* L = (LAS char*)smem;
  {
    unsigned long long pc0;
    asm volatile("s_getpc_b64 %0" : "=s"(pc0));
    const unsigned voff = threadIdx.x * 16;
    const unsigned ldst = (unsigned)(size_t)L + 131072 + 8192;
    unsigned keep;
    asm volatile("s_mov_b32 %0, m0\n\ts_mov_b32 m0, %3\n\ts_nop 0\n\t"
                 "global_load_lds_dwordx4 %1, %2\n\t"
                 "global_load_lds_dwordx4 %1, %4\n\t"
                 "global_load_lds_dwordx4 %1, %5\n\t"
                 "s_mov_b32 m0, %0"
                 : "=&s"(keep) : "v"(voff), "s"(pc0 & ~255ull), "s"(ldst), "s"((pc0 & ~255ull) + 8192), "s"((pc0 & ~255ull) + 16384) : "memory");
  }
  const int tid = threadIdx.x, lane = tid & 63, w = __builtin_amdgcn_readfirstlane(tid >> 6);
  const int xcd = blockIdx.x & 7, idx = blockIdx.x >> 3;
  const bool roleW = idx < 16;
  const int j = idx & 15;
  const int g = lane >> 4, l15 = lane & 15;
  STAMP(0);
  const unsigned long long t_start = __builtin_amdgcn_s_memrealtime();
  const __amdgpu_buffer_rsrc_t rs = __builtin_amdgcn_make_buffer_rsrc(ws, 0, WS_TOTAL, 0x00020000);

  if (__builtin_expect(roleW, 1)) {
    constexpr int L_RED = 0, L_QS = 8192, L_Q2 = 9216, L_CNTW = 10240, L_CCB = 12288, L_RED2 = 17408, L_CX = 26624, L_STG = 36864, L_FRW = 53248, L_WK = 65536;
    const int h = xcd * 4 + (j >> 2), j4 = j & 3, ch = j4 >> 1, dh = j4 & 1, pw = xcd * 16 + j;
    const int fgrp = xcd * 2 + (j >> 3), fkq = j & 7, fi = fgrp * 8 + fkq;
    const bool ffn = fgrp < 14;
    const unsigned ep_v = __hip_atomic_load((GAS unsigned*)(ws + WS_EPOCH), RLX_AGENT);
    unsigned ep;
    const int ks2 = (lane >> 3) & 1, nb8 = lane & 7;
    v4u wa[8], wb[8];
#define LOAD_WO() _Pragma("unroll") for (int r = 0; r < 8; ++r) wb[r] = ldg(wo + (size_t)(h * 128 + 64 * dh + 32 * ks2 + 8 * g + r) * 512 + 64 * w + 8 * nb8)
    if (__builtin_expect(w >= 4, 1)) {
      const int m0 = 128 * j4 + 32 * (w - 4);
      v4u wt[8], xs[4];
#pragma unroll
      for (int k = 0; k < 4; ++k) xs[k] = ldx(x + g * 512 + l15 * 32 + k * 8);
      const v4u xf = ldx(x + (lane & 3) * 512 + m0 + 8 * g);
      const int k0 = 64 * fkq + 32 * (w & 1);
      const v4u fx = ldx(x + (lane & 3) * 512 + k0 + 8 * g);
#pragma unroll
      for (int r = 0; r < 8; ++r) wt[r] = ldg(wq + (size_t)(m0 + 8 * g + r) * 4096 + h * 128 + 8 * l15);
      PIN();
      STAMP(3);
      const Ln ln = ln_stats(xs);
      ep = __builtin_amdgcn_readfirstlane(ep_v) + 1u;
      const int rb = lane & 3;
      const float mb = sel4(rb, ln.mb0, ln.mb1, ln.mb2, ln.mb3), den = sel4(rb, ln.d0, ln.d1, ln.d2, ln.d3);
      const v4u a = xn_frag(xf, mb, den), a0 = xn_frag(fx, mb, den);
      PIN();
      STAMP(5);
      v4u t[8];
      transpose8(wt, t);
      gemv8_store(a, t, L, L_RED + ((w - 4) * 4 * 128 + 8 * lane) * 4, 512, lane < 16);
      STAMP(11);
      BAR();
      BAR();
      BAR();
      if (ffn) {
        const u16* wsrc = (w < 6 ? wg : wu) + (size_t)(k0 + 8 * g) * 1792 + 128 * fgrp + 8 * l15;
        v4u wf[8];
#pragma unroll
        for (int r = 0; r < 8; ++r) wf[r] = ldg(wsrc + (size_t)r * 1792);
        PIN();
        LOAD_WO();
        PIN();
        transpose8(wf, t);
        gemv8_store(a0, t, L, L_FRW + ((w - 4) * 512 + 8 * lane) * 4, 512, lane < 16);
        LCNT_ADD(L_CNTW);
        LCNT_WAIT(L_CNTW, 4u);
#pragma unroll
        for (int k = 0; k < 2; ++k) {
          const int mat = (w - 4) >> 1, bb = 2 * (w & 1) + k;
          const f32x2 v = *(LAS f32x2*)(L + L_FRW + ((2 * mat) * 512 + bb * 128 + 2 * lane) * 4) + *(LAS f32x2*)(L + L_FRW + ((2 * mat + 1) * 512 + bb * 128 + 2 * lane) * 4);
          st_sc1(v4u{__float_as_uint(v.x), ep, __float_as_uint(v.y), ep}, rs, WS_F1 + (((fi * 2 + mat) * 4 + bb) * 128 + 2 * lane) * 8);
        }
        PIN();
      } else {
        LOAD_WO();
        PIN();
      }
    } else {
      const int c0 = 128 * j4 + 32 * w;
      if (tid == 0) *(LAS v2u*)(L + L_CNTW) = v2u{0u, 0u};
      ep = __builtin_amdgcn_readfirstlane(ep_v) + 1u;
      PIN();
#pragma unroll
      for (int i = 0; i < 8; ++i) {
        const int row = 4 * i + g;
        __builtin_amdgcn_global_load_lds((const GAS unsigned*)(wuk + (size_t)(c0 + row) * 4096 + h * 128 + (l15 ^ (row & 15)) * 8),
                                         (LAS unsigned*)(L + L_WK + w * 8192 + i * 1024), 16, 0, 2);
      }
      PIN();
      BAR();
      STAMP(1);
      {
        f32x2 s = {0.f, 0.f};
#pragma unroll
        for (int k = 0; k < 4; ++k) s += *(LAS f32x2*)(L + L_RED + ((k * 4 + w) * 128 + 2 * lane) * 4);
        st_sc1(v4u{__float_as_uint(s.x), ep, __float_as_uint(s.y), ep}, rs, WS_H0 + (((h * 4 + j4) * 4 + w) * 64 + lane) * 16);
      }
      PIN();
      BAR();
      asm volatile("s_waitcnt vmcnt(1)" ::: "memory");
      v4u wk[8];
#pragma unroll
      for (int rt = 0; rt < 2; ++rt)
#pragma unroll
        for (int ks = 0; ks < 4; ++ks) wk[rt * 4 + ks] = lds_r128(L, L_WK + w * 8192 + (16 * rt + l15) * 256 + 16 * ((4 * ks + g) ^ l15));
      __builtin_amdgcn_s_sleep(H0_DELAY);
      v4u v[4];
      for (unsigned spins = 0;; ++spins) {
        asm volatile("" ::: "memory");
        bool ok = true;
#pragma unroll
        for (int mq = 0; mq < 4; ++mq) v[mq] = ld_sc1(rs, WS_H0 + (((h * 4 + mq) * 4 + w) * 64 + lane) * 16);
#pragma unroll
        for (int mq = 0; mq < 4; ++mq) ok &= (v[mq].y == ep) & (v[mq].w == ep);
        if (__all(ok) || spins > SPIN_CAP) break;
        __builtin_amdgcn_s_sleep(2);
      }
      if (tid == 0) st_sc1(v4u{ep, ep, ep, ep}, rs, WS_HINT1 + pw * 128);
      STAMP(12);
      const float q0 = (__uint_as_float(v[0].x) + __uint_as_float(v[1].x)) + (__uint_as_float(v[2].x) + __uint_as_float(v[3].x));
      const float q1 = (__uint_as_float(v[0].z) + __uint_as_float(v[1].z)) + (__uint_as_float(v[2].z) + __uint_as_float(v[3].z));
      *(LAS unsigned*)(L + L_QS + (w * 128 + 2 * lane) * 2) = pk_bf16(q0, q1);
      LCNT_ADD(L_CNTW + 4);
      LCNT_WAIT(L_CNTW + 4, 4u);
      v4u bq[4];
#pragma unroll
      for (int ks = 0; ks < 4; ++ks) bq[ks] = lds_r128(L, L_QS + ((lane & 3) * 128 + 32 * ks + 8 * g) * 2);
#pragma unroll
      for (int rt = 0; rt < 2; ++rt) {
        f32x4 d = {0.f, 0.f, 0.f, 0.f};
#pragma unroll
        for (int ks = 0; ks < 4; ++ks) d = mfma16(wk[rt * 4 + ks], bq[ks], d);
        if (l15 < 4) *(LAS v2u*)(L + L_Q2 + w * 256 + l15 * 64 + (4 * rt + g) * 8) = v2u{pk_bf16(d[0], d[1]), pk_bf16(d[2], d[3])};
      }
      asm volatile("s_waitcnt lgkmcnt(0)" ::: "memory");
      if (lane < 32) {
        const v2u d = *(LAS v2u*)(L + L_Q2 + w * 256 + lane * 8);
        st_sc1(v4u{d.x, ep, d.y, ep}, rs, WS_H1 + (((lane >> 3) * 32 + h) * 128 + (c0 >> 2) + (lane & 7)) * 16);
      }
      STAMP(14);
      PIN();
      BAR();
      __builtin_amdgcn_s_sleep(2);
#pragma unroll
      for (int r = 0; r < 8; ++r) wa[r] = ldg(wuv + (size_t)(256 * ch + 64 * w + 32 * ks2 + 8 * g + r) * 4096 + h * 128 + 64 * dh + 8 * nb8);
      LOAD_WO();
      PIN();
    }
    STAMP(6);
    v4u wvT[8], woT[8];
    {
      if (w < 4) transpose8(wa, wvT);
      transpose8(wb, woT);
#pragma unroll
      for (int i = 0; i < 8; ++i) asm volatile("" : "+v"(woT[i]), "+v"(wvT[i]));
    }
    STAMP(7);
    const int bb = w >> 1;
    {
      {
        v4u t1;
        for (unsigned spins = 0; spins <= SPIN_CAP; ++spins) {
          asm volatile("" ::: "memory");
          t1 = ld_sc1(rs, WS_HINT2 + (bb * 32 + (lane & 31)) * 128);
          if (__all((t1.x == ep) & (t1.z == ep))) break;
          __builtin_amdgcn_s_sleep(POLL_SLEEP);
        }
        while (__any((unsigned)__builtin_amdgcn_s_memrealtime() - t1.y < H2_WAIT)) __builtin_amdgcn_s_sleep(1);
      }
      STAMP(8);
      v4u p[8], s;
      const int base = WS_H2P + (((bb * 32 + g) * 32 + h) * 512 + 256 * ch + 128 * (w & 1) + 8 * l15) * 2;
      for (unsigned spins = 0;; ++spins) {
        asm volatile("" ::: "memory");
        s = ld_sc1(rs, WS_H2S + ((bb * 32 + (lane & 31)) * 32 + h) * 16);
#pragma unroll
        for (int i = 0; i < 8; ++i) p[i] = ld_sc1(rs, base + (i ^ (4 * dh)) * (4 * 32 * 512 * 2));
        bool ok = (s.y == ep) & (s.w == ep);
#pragma unroll
        for (int i = 0; i < 8; ++i) ok &= (p[i].x != SENT) & (p[i].y != SENT) & (p[i].z != SENT) & (p[i].w != SENT);
        const bool allok = __all(ok);
        if (spins == 0) STAMP(2);
        if (allok || spins > SPIN_CAP) break;
        __builtin_amdgcn_s_sleep(2);
      }
      float wgt;
      {
        const float m = __uint_as_float(s.x), l = __uint_as_float(s.z);
        const float Mx = x16_max(row16_max(m));
        const float e = __builtin_amdgcn_exp2f(m - Mx);
        wgt = e * __builtin_amdgcn_rcpf(x16_sum(row16_sum(e * l)));
      }
      if (tid == 0) st_sc1(v4u{ep, ep, ep, ep}, rs, WS_HINT3 + pw * 128);
      STAMP(15);
      float acc[8] = {0.f, 0.f, 0.f, 0.f, 0.f, 0.f, 0.f, 0.f};
#pragma unroll
      for (int i = 0; i < 8; ++i) {
        const int s0 = 4 * (i ^ (4 * dh));
        const float ww = sel4(g, rdlane(wgt, s0), rdlane(wgt, s0 + 1), rdlane(wgt, s0 + 2), rdlane(wgt, s0 + 3));
#pragma unroll
        for (int e = 0; e < 4; ++e) { acc[2 * e] += ww * bf_lo(p[i][e]); acc[2 * e + 1] += ww * bf_hi(p[i][e]); }
      }
#pragma unroll
      for (int e = 0; e < 8; ++e) acc[e] = x32_sum(x16_sum(acc[e]));
      if (lane < 16)
        *(LAS v4u*)(L + L_CCB + bb * 576 + (128 * (w & 1) + 8 * lane) * 2) =
            v4u{pk_bf16(acc[0], acc[1]), pk_bf16(acc[2], acc[3]), pk_bf16(acc[4], acc[5]), pk_bf16(acc[6], acc[7])};
    }
    BAR();
    STAMP(9);
    const int rb = lane & 3, rks = (lane >> 2) & 1;
    const bool dvalid = g == ks2;
    if (w < 4) {
      const v4u a = lds_r128(L, L_CCB + rb * 576 + (64 * w + 32 * rks + 8 * g) * 2);
      gemv8_store(a, wvT, L, L_RED2 + ((w * 2 + ks2) * 4 * 64 + 8 * nb8) * 4, 256, dvalid);
    }
    BAR();
    {
      f32x4 a0 = {0.f, 0.f, 0.f, 0.f};
#pragma unroll
      for (int p = 0; p < 8; ++p) a0 += lds_rf4(L, L_RED2 + p * 1024 + lane * 16);
      *(LAS v2u*)(L + L_CX + w * 512 + lane * 8) = v2u{pk_bf16(a0[0], a0[1]), pk_bf16(a0[2], a0[3])};
      asm volatile("s_waitcnt lgkmcnt(0)" ::: "memory");
      const v4u a = lds_r128(L, L_CX + w * 512 + rb * 128 + (32 * rks + 8 * g) * 2);
      gemv8_store(a, woT, L, L_STG + ((w * 2 + ks2) * 4 * 64 + 8 * nb8) * 4, 256, dvalid);
      asm volatile("s_waitcnt lgkmcnt(0)" ::: "memory");
#pragma unroll
      for (int k = 0; k < 2; ++k) {
        const f32x2 v = *(LAS f32x2*)(L + L_STG + w * 2048 + k * 512 + lane * 8) + *(LAS f32x2*)(L + L_STG + w * 2048 + 1024 + k * 512 + lane * 8);
        st_sc1(v4u{__float_as_uint(v.x), ep, __float_as_uint(v.y), ep}, rs, WS_H3S + ((pw * 4 + 2 * k + (lane >> 5)) * 512 + 64 * w + 2 * (lane & 31)) * 8);
      }
    }
    STAMP(13);
      DUMP();
    return;
  }
  {
    const int b = xcd >> 1, slice = (xcd & 1) * 16 + j;
    const int fgrp = xcd * 2 + (j >> 3), fkq = j & 7, fi = fgrp * 8 + fkq;
    const bool ffn = fgrp < 14;
    unsigned ep_v;
    asm volatile("global_load_dword %0, %1, off sc1" : "=v"(ep_v) : "v"(ws + WS_EPOCH) : "memory");
    while (__builtin_amdgcn_s_memrealtime() - t_start < A_DELAY) __builtin_amdgcn_s_sleep(1);
    PIN();
    {
      const u16* ksrc = ckv + ((size_t)(b * 4096 + slice * 128 + 16 * w)) * 512 + g * 128;
#pragma unroll
      for (int i = 0; i < 16; ++i) {
        if (i == A_FIRST) { PIN(); while (__builtin_amdgcn_s_memrealtime() - t_start < A_SECOND) __builtin_amdgcn_s_sleep(1); PIN(); }
        __builtin_amdgcn_global_load_lds((const GAS unsigned*)(ksrc + (size_t)i * 512 + (l15 ^ swz(i)) * 8),
                                         (LAS unsigned*)(L + (16 * w + i) * 1024), 16, 0, 2);
      }
    }
    PIN();
    __builtin_amdgcn_s_barrier();
    const int pofs = WS_H2P + (((b * 32 + slice) * 32 + (lane >> 3)) * 512 + 64 * w + 8 * (lane & 7)) * 2;
#pragma unroll
    for (int i = 0; i < 4; ++i) st_sc1(v4u{SENT, SENT, SENT, SENT}, rs, pofs + i * 8192);
    PIN();
    asm volatile("s_waitcnt vmcnt(4)" : "+v"(ep_v) :: "memory");
    const unsigned ep = __builtin_amdgcn_readfirstlane(ep_v) + 1u;
    v4u ka[16];
    v2u tr[4][4][2];
    const unsigned lb = (unsigned)(size_t)L;
    {
      const int krow = 8 * ((l15 >> 2) & 1) + 4 * (l15 >> 3) + (l15 & 3);
      const int sk = swz(krow);
#pragma unroll
      for (int k4 = 0; k4 < 4; ++k4) {
        const unsigned addr = lb + (16 * w + krow) * 1024 + 16 * ((4 * k4 + g) ^ sk);
#pragma unroll
        for (int ct = 0; ct < 4; ++ct) asm volatile("ds_read_b128 %0, %1 offset:%2" : "=v"(ka[ct * 4 + k4]) : "v"(addr), "i"(ct * 256) : "memory");
      }
    }
    BAR();
    STAMP(10);
    {
      const int q4 = l15 >> 2, p4 = lane & 3;
      const unsigned row = 8 * (g & 1) + 4 * (g >> 1) + q4;
#pragma unroll
      for (int u = 0; u < 2; ++u) {
        const unsigned addr = lb + row * 1024 + (w >> 1) * 256 + 16 * ((8 * (w & 1) + 4 * u + p4) ^ swz(row));
#pragma unroll
        for (int s = 0; s < 4; ++s)
#pragma unroll
          for (int t2 = 0; t2 < 2; ++t2)
#pragma unroll
            for (int e = 0; e < 2; ++e)
              asm volatile("ds_read_b64_tr_b16 %0, %1 offset:%2" : "=v"(tr[s][u * 2 + e][t2]) : "v"(addr + (s >> 1) * 65536), "i"((s & 1) * 32768 + t2 * 16384 + 8 * e) : "memory");
      }
    }
    {
      const int qbase = WS_H1 + ((b * 32 + 4 * w) * 128 + lane) * 16;
      for (unsigned spins = 0; spins <= SPIN_CAP; ++spins) {
        asm volatile("" ::: "memory");
        const v4u t1 = ld_sc1(rs, WS_HINT1 + (16 * w + l15) * 128);
        if (__all((t1.x == ep) & (t1.z == ep))) { if (spins) __builtin_amdgcn_s_sleep(H1_DELAY); break; }
        __builtin_amdgcn_s_sleep(POLL_SLEEP);
      }
      STAMP(2);
      v4u qv[8];
      for (unsigned spins = 0;; ++spins) {
        asm volatile("" ::: "memory");
#pragma unroll
        for (int i = 0; i < 8; ++i) qv[i] = ld_sc1(rs, qbase + i * 1024);
        bool ok = true;
#pragma unroll
        for (int i = 0; i < 8; ++i) ok &= (qv[i].y == ep) & (qv[i].w == ep);
        if (__all(ok) || spins > SPIN_CAP) break;
        __builtin_amdgcn_s_sleep(2);
      }
      STAMP(11);
      const int gq = (lane >> 1) & 3;
#pragma unroll
      for (int i = 0; i < 8; ++i) {
        const int h16 = (4 * w + (i >> 1)) & 15, ks = 8 * (i & 1) + (lane >> 3);
        *(LAS v2u*)(L + L_QA + (w >> 2) * 16384 + ks * 1024 + ((h16 ^ (gq | ((ks & 1) << 2))) + 16 * gq) * 16 + 8 * (lane & 1)) = v2u{qv[i].x, qv[i].z};
      }
    }
    VM0();
    BAR();
#pragma unroll
    for (int s = 0; s < 4; ++s) {
      asm volatile("" : "+v"(tr[s][0][0]), "+v"(tr[s][0][1]), "+v"(tr[s][1][0]), "+v"(tr[s][1][1]), "+v"(tr[s][2][0]), "+v"(tr[s][2][1]), "+v"(tr[s][3][0]), "+v"(tr[s][3][1]));
      asm volatile("" : "+v"(ka[4 * s]), "+v"(ka[4 * s + 1]), "+v"(ka[4 * s + 2]), "+v"(ka[4 * s + 3]));
    }
    if (tid == 0) st_sc1(v4u{ep, (unsigned)__builtin_amdgcn_s_memrealtime(), ep, 0u}, rs, WS_HINT2 + (b * 32 + slice) * 128);
    STAMP(3);
    float sc[2][4];
    {
      constexpr int RING = 16;
      v4u bq[RING];
      const unsigned qa0 = (unsigned)(size_t)L + L_QA + ((l15 ^ g) + 16 * g) * 16, qa1 = (unsigned)(size_t)L + L_QA + ((l15 ^ (g | 4)) + 16 * g) * 16;
#define QREAD(i) asm volatile("ds_read_b128 %0, %1 offset:%2" : "=v"(bq[(i) % RING]) : "v"((((i) >> 1) & 1) ? qa1 : qa0), "i"(((i) & 1) * 16384 + ((i) >> 1) * 1024) : "memory")
#pragma unroll
      for (int i = 0; i < RING; ++i) QREAD(i);
      f32x4 acc[2] = {{0.f, 0.f, 0.f, 0.f}, {0.f, 0.f, 0.f, 0.f}};
#pragma unroll
      for (int i = 0; i < 32; i += 2) {
        asm volatile("s_waitcnt lgkmcnt(%2)" : "+v"(bq[i % RING]), "+v"(bq[(i + 1) % RING]) : "i"(i + RING <= 32 ? RING - 2 : 30 - i) : "memory");
        acc[0] = mfma16(ka[i >> 1], bq[i % RING], acc[0]);
        acc[1] = mfma16(ka[i >> 1], bq[(i + 1) % RING], acc[1]);
        __builtin_amdgcn_sched_barrier(0);
        if (i + RING < 32) { QREAD(i + RING); QREAD(i + RING + 1); }
      }
      constexpr float SC = 0.08838834764831845f * 1.4426950408889634f;
      float m[2];
#pragma unroll
      for (int hh = 0; hh < 2; ++hh) {
#pragma unroll
        for (int e = 0; e < 4; ++e) sc[hh][e] = acc[hh][e] * SC;
        m[hh] = fmaxf(fmaxf(sc[hh][0], sc[hh][1]), fmaxf(sc[hh][2], sc[hh][3]));
      }
      auto r1 = __builtin_amdgcn_permlane16_swap(__float_as_uint(m[0]), __float_as_uint(m[1]), false, false);
      const float mm = x32_max(fmaxf(__uint_as_float(r1[0]), __uint_as_float(r1[1])));
      if (lane < 32) *(LAS float*)(L + L_MX + (lane * 8 + w) * 4) = mm;
    }
    BAR();
    STAMP(4);
    float M[2];
    {
      f32x4 mx[2][2];
#pragma unroll
      for (int hh = 0; hh < 2; ++hh) { mx[hh][0] = lds_rf4(L, L_MX + (hh * 16 + l15) * 32); mx[hh][1] = lds_rf4(L, L_MX + (hh * 16 + l15) * 32 + 16); }
#pragma unroll
      for (int hh = 0; hh < 2; ++hh) {
        M[hh] = fmaxf(fmaxf(fmaxf(mx[hh][0][0], mx[hh][0][1]), fmaxf(mx[hh][0][2], mx[hh][0][3])), fmaxf(fmaxf(mx[hh][1][0], mx[hh][1][1]), fmaxf(mx[hh][1][2], mx[hh][1][3])));
        const v2u p = {pk_bf16(__builtin_amdgcn_exp2f(sc[hh][0] - M[hh]), __builtin_amdgcn_exp2f(sc[hh][1] - M[hh])),
                       pk_bf16(__builtin_amdgcn_exp2f(sc[hh][2] - M[hh]), __builtin_amdgcn_exp2f(sc[hh][3] - M[hh]))};
        *(LAS v2u*)(L + L_P + hh * 4096 + ((w >> 1) * 64 + lane) * 16 + 8 * (w & 1)) = p;
      }
    }
    BAR();
    STAMP(5);
    {
      v4u pb[2][4];
#pragma unroll
      for (int hh = 0; hh < 2; ++hh)
#pragma unroll
        for (int s = 0; s < 4; ++s) pb[hh][s] = lds_r128(L, L_P + hh * 4096 + (s * 64 + lane) * 16);
      if (w == 0) {
        const v4u ones = {0x3F803F80u, 0x3F803F80u, 0x3F803F80u, 0x3F803F80u};
        f32x4 l0 = {0.f, 0.f, 0.f, 0.f}, l1 = {0.f, 0.f, 0.f, 0.f};
#pragma unroll
        for (int s = 0; s < 4; ++s) { l0 = mfma16(ones, pb[0][s], l0); l1 = mfma16(ones, pb[1][s], l1); }
        if (lane < 32)
          st_sc1(v4u{__float_as_uint(lane < 16 ? M[0] : M[1]), ep, __float_as_uint(lane < 16 ? l0[0] : l1[0]), ep}, rs, WS_H2S + ((b * 32 + slice) * 32 + lane) * 16);
      }
#pragma unroll
      for (int hh = 0; hh < 2; ++hh) {
        f32x4 o[4];
#pragma unroll
        for (int c = 0; c < 4; ++c) o[c] = f32x4{0.f, 0.f, 0.f, 0.f};
#pragma unroll
        for (int s = 0; s < 4; ++s)
#pragma unroll
          for (int c = 0; c < 4; ++c) o[c] = mfma16(v4u{tr[s][c][0].x, tr[s][c][0].y, tr[s][c][1].x, tr[s][c][1].y}, pb[hh][s], o[c]);
#pragma unroll
        for (int u = 0; u < 2; ++u) {
          v4u v = {pk_bf16(o[2 * u][0], o[2 * u][1]), pk_bf16(o[2 * u][2], o[2 * u][3]), pk_bf16(o[2 * u + 1][0], o[2 * u + 1][1]), pk_bf16(o[2 * u + 1][2], o[2 * u + 1][3])};
#pragma unroll
          for (int e = 0; e < 4; ++e) v[e] = v[e] == SENT ? 0x7FC07FC0u : v[e];
          *(LAS v4u*)(L + L_OST + w * 4096 + (hh * 16 + l15) * 128 + 16 * ((4 * u + g) ^ (l15 & 7))) = v;
        }
#pragma unroll
        for (int i = 2 * hh; i < 2 * hh + 2; ++i) {
          const int hr = 8 * i + (lane >> 3);
          st_sc1(lds_r128(L, L_OST + w * 4096 + hr * 128 + 16 * ((lane & 7) ^ (hr & 7))), rs, pofs + i * 8192);
        }
      }
    }
    PIN();
    __builtin_amdgcn_s_barrier();
    STAMP(6);
    if (ffn && (w == 1 || w == 2)) {
      v4u wf[8];
      const int wd2 = w - 1;
#pragma unroll
      for (int r = 0; r < 8; ++r) wf[r] = ldg(wd + (size_t)(16 * fi + 8 * (g & 1) + r) * 512 + 256 * wd2 + 128 * (g >> 1) + 8 * l15);
      v4u gv[8];
      const int gofs = WS_F1 + ((fgrp * 8 * 8 + (lane >> 3)) * 128 + 16 * fkq + 2 * (lane & 7)) * 8;
      for (unsigned spins = 0;; ++spins) {
        asm volatile("" ::: "memory");
        bool ok = true;
#pragma unroll
        for (int k = 0; k < 8; ++k) gv[k] = ld_sc1(rs, gofs + k * 8192);
#pragma unroll
        for (int k = 0; k < 8; ++k) ok &= (gv[k].y == ep) & (gv[k].w == ep);
        if (__all(ok) || spins > SPIN_CAP) break;
        __builtin_amdgcn_s_sleep(8);
      }
      float s0 = 0.f, s1 = 0.f;
#pragma unroll
      for (int k = 0; k < 8; ++k) { s0 += __uint_as_float(gv[k].x); s1 += __uint_as_float(gv[k].z); }
      s0 = rbf(s0); s1 = rbf(s1);
      float g0, u0, g1, u1;
      x32_both(s0, g0, u0);
      x32_both(s1, g1, u1);
      if (lane < 32) *(LAS unsigned*)(L + L_HM + wd2 * 128 + lane * 4) = pk_bf16(rbf(silu_f(g0)) * u0, rbf(silu_f(g1)) * u1);
      asm volatile("s_waitcnt lgkmcnt(0)" ::: "memory");
      v4u a = lds_r128(L, L_HM + wd2 * 128 + ((lane & 3) * 16 + 8 * (g & 1)) * 2);
      if ((g >> 1) != ((lane >> 2) & 1)) a = v4u{0u, 0u, 0u, 0u};
      v4u t[8];
      transpose8(wf, t);
      const int sb = L_FS + wd2 * 4096;
      gemv8_store(a, t, L, sb + (128 * g + 8 * l15) * 4, 1024, g < 2);
      asm volatile("s_waitcnt lgkmcnt(0)" ::: "memory");
#pragma unroll
      for (int k = 0; k < 8; ++k) {
        const f32x2 v = *(LAS f32x2*)(L + sb + k * 512 + lane * 8);
        st_sc1(v4u{__float_as_uint(v.x), ep, __float_as_uint(v.y), ep}, rs,
               WS_H3Y + ((fi * 4 + (k >> 1)) * 512 + 256 * wd2 + 128 * (k & 1) + 2 * lane) * 8);
      }
    }
    STAMP(7);
    const int aj = xcd * 16 + j, ob = aj >> 5, om0 = (aj & 31) * 16;
    float rsel = 0.f, mbo = 0.f, deno = 0.f;
    unsigned xv = 0;
    if (w == 0) {
      v4u wrv[4], xs[4];
#pragma unroll
      for (int k = 0; k < 4; ++k) xs[k] = ldx(x + g * 512 + l15 * 32 + k * 8);
#pragma unroll
      for (int k = 0; k < 4; ++k) wrv[k] = ldx(wr + l15 * 32 + k * 8);
      xv = *(const GAS unsigned*)(x + ob * 512 + om0 + 2 * (lane & 7));
      const Ln ln = ln_stats(xs);
      mbo = sel4(ob, ln.mb0, ln.mb1, ln.mb2, ln.mb3);
      deno = sel4(ob, ln.d0, ln.d1, ln.d2, ln.d3);
      const float mb = sel4(g, ln.mb0, ln.mb1, ln.mb2, ln.mb3), den = sel4(g, ln.d0, ln.d1, ln.d2, ln.d3);
      float r = 0.f;
#pragma unroll
      for (int k = 0; k < 4; ++k)
#pragma unroll
        for (int e = 0; e < 4; ++e) { r += xn1(bf_lo(xs[k][e]), mb, den) * bf_lo(wrv[k][e]); r += xn1(bf_hi(xs[k][e]), mb, den) * bf_hi(wrv[k][e]); }
      r = rbf(row16_sum(r));
      r = rbf(1.f / (1.f + __expf(-r)));
      rsel = rdlane(r, 16 * ob);
    }
    {
      const int piece = tid >> 3, sub = tid & 7;
      const bool yv = piece < 48;
      const int o0s = WS_H3S + ((piece * 4 + ob) * 512 + om0 + 2 * sub) * 8;
      const int oy = WS_H3Y + ((piece * 4 + ob) * 512 + om0 + 2 * sub) * 8;
      const int oy2 = oy + (yv ? 64 * 4 * 512 * 8 : 0);
      for (unsigned spins = 0; spins <= SPIN_CAP; ++spins) {
        asm volatile("" ::: "memory");
        const v4u t1 = ld_sc1(rs, WS_HINT3 + (8 * w + (lane & 7) + 64 * ((lane >> 3) & 1)) * 128);
        if (__all((t1.x == ep) & (t1.z == ep))) break;
        __builtin_amdgcn_s_sleep(POLL_SLEEP);
      }
      STAMP(8);
      v4u a, c, y0, y1;
      for (unsigned spins = 0;; ++spins) {
        asm volatile("" ::: "memory");
        y0 = ld_sc1(rs, oy);
        y1 = ld_sc1(rs, oy2);
        if (__all((y0.y == ep) & (y0.w == ep) & (y1.y == ep) & (y1.w == ep)) || spins > SPIN_CAP) break;
        __builtin_amdgcn_s_sleep(POLL_SLEEP);
      }
      __builtin_amdgcn_s_sleep(H3_DELAY);
      for (unsigned spins = 0;; ++spins) {
        asm volatile("" ::: "memory");
        a = ld_sc1(rs, o0s);
        c = ld_sc1(rs, o0s + 64 * 4 * 512 * 8);
        if (__all((a.y == ep) & (a.w == ep) & (c.y == ep) & (c.w == ep)) || spins > SPIN_CAP) break;
        __builtin_amdgcn_s_sleep(2);
      }
      f32x4 v = {__uint_as_float(a.x) + __uint_as_float(c.x), __uint_as_float(a.z) + __uint_as_float(c.z),
                 __uint_as_float(y0.x) + (yv ? __uint_as_float(y1.x) : 0.f), __uint_as_float(y0.z) + (yv ? __uint_as_float(y1.z) : 0.f)};
#pragma unroll
      for (int e = 0; e < 4; ++e) v[e] = x32_sum(x16_sum(v[e] + dpp<0x128>(v[e])));
      if (lane < 8) lds_wf4(L, L_SR + (w * 8 + lane) * 16, v);
      BAR();
      STAMP(9);
      if (w == 0) {
        f32x4 t = {0.f, 0.f, 0.f, 0.f};
#pragma unroll
        for (int k = 0; k < 8; ++k) t += lds_rf4(L, L_SR + (k * 8 + (lane & 7)) * 16);
        if (lane < 8) {
          const float mb = mbo, den = deno;
          float o2[2];
#pragma unroll
          for (int e = 0; e < 2; ++e) {
            const float xx = e ? bf_hi(xv) : bf_lo(xv);
            const float yb = rbf(rbf(t[2 + e]) * rsel);
            const float z = rbf(xn1(xx, mb, den) + yb);
            const float gg = rbf(silu_f(z));
            o2[e] = xx + rbf(rbf(t[e]) * gg);
          }
          *(GAS unsigned*)(out + ob * 512 + om0 + 2 * lane) = pk_bf16(o2[0], o2[1]);
        }
        if (aj == 0 && lane == 0) __hip_atomic_store((GAS unsigned*)(ws + WS_EPOCH), ep, RLX_AGENT);
      }
    }
    STAMP(13);
    DUMP();
  }
}

__global__ void k_empty(int* p) { if (p == nullptr) *p = 0; }
extern "C" void kernel_launch(void* const* d_in, const int* in_sizes, int n_in, void* d_out, int out_size, void* d_ws, size_t ws_size,
                              hipStream_t stream) {
  (void)hipFuncSetAttribute((const void*)mla_moe_kernel, hipFuncAttributeMaxDynamicSharedMemorySize, L_TOTAL);
  mla_moe_kernel<<<256, 512, L_TOTAL, stream>>>((const u16*)d_in[0], (const u16*)d_in[1], (const u16*)d_in[2], (const u16*)d_in[3],
                                                (const u16*)d_in[4], (const u16*)d_in[5], (const u16*)d_in[7], (const u16*)d_in[8],
                                                (const u16*)d_in[9], (const u16*)d_in[10], (u16*)d_out, (char*)d_ws);
  k_empty<<<1, 64, 0, stream>>>((int*)d_ws);
}
```

```cpp
#include <hip/hip_runtime.h>
#include <hip/hip_bf16.h>
#include <stdint.h>

typedef unsigned short u16;
typedef unsigned v4u __attribute__((ext_vector_type(4)));
typedef unsigned v2u __attribute__((ext_vector_type(2)));
typedef float f32x2 __attribute__((ext_vector_type(2)));
typedef float f32x4 __attribute__((ext_vector_type(4)));
typedef short bf16x8 __attribute__((ext_vector_type(8)));
typedef __bf16 bf2 __attribute__((ext_vector_type(2)));
#define DI __device__ __forceinline__
#define GAS __attribute__((address_space(1)))
#define LAS __attribute__((address_space(3)))
#define RLX_AGENT __ATOMIC_RELAXED, __HIP_MEMORY_SCOPE_AGENT

constexpr int WS_EPOCH = 0;
constexpr int WS_HINT1 = 16384;
constexpr int WS_HINT3 = 32768;
constexpr int WS_HINT2 = 49152;
constexpr int WS_H0 = 65536;
constexpr int WS_H1 = WS_H0 + 512 * 1024;
constexpr int WS_H2S = WS_H1 + 256 * 1024;
constexpr int WS_H3S = WS_H2S + 64 * 1024;
constexpr int WS_H3Y = WS_H3S + 2048 * 1024;
constexpr int WS_F1 = WS_H3Y + 2048 * 1024;
constexpr int WS_H2P = WS_F1 + 1024 * 1024;
constexpr int WS_TOTAL = WS_H2P + 4096 * 1024;
constexpr unsigned SPIN_CAP = 1u << 17;
constexpr unsigned SENT = 0xFFFFFFFFu;
constexpr int A_FIRST = 4;
constexpr unsigned A_SECOND = 200;
constexpr int H0_DELAY = 16;
constexpr unsigned H1_GATE = 400, H1_WAIT = 200;
constexpr unsigned H2_GATE = 340, H2_WAIT = 125;
constexpr unsigned H3_GATE = 280, H3_WAIT = 200;
constexpr int POLL_SLEEP = 8;
constexpr int CODE_KB = 24;

constexpr int L_QA = 131072;
constexpr int L_MX = 0;
constexpr int L_P = 2048;
constexpr int L_SR = 16384;
constexpr int L_HM = 17408;
constexpr int L_FS = 20480;
constexpr int L_OST = 32768;
constexpr int L_TOTAL = L_QA + 32768;

DI float bf_lo(unsigned u) { return __uint_as_float(u << 16); }
DI float bf_hi(unsigned u) { return __uint_as_float(u & 0xFFFF0000u); }
DI unsigned pk_bf16(float lo, float hi) { f32x2 v = {lo, hi}; return __builtin_bit_cast(unsigned, __builtin_convertvector(v, bf2)); }
DI float rbf(float f) { return bf_lo(pk_bf16(f, f)); }
DI f32x4 mfma16(v4u a, v4u b, f32x4 c) { return __builtin_amdgcn_mfma_f32_16x16x32_bf16(__builtin_bit_cast(bf16x8, a), __builtin_bit_cast(bf16x8, b), c, 0, 0, 0); }
template <int CTRL> DI float dpp(float v) { return __builtin_bit_cast(float, __builtin_amdgcn_update_dpp(0, __builtin_bit_cast(int, v), CTRL, 0xF, 0xF, true)); }
DI float row16_sum(float v) { v += dpp<0xB1>(v); v += dpp<0x4E>(v); v += dpp<0x141>(v); v += dpp<0x140>(v); return v; }
DI float row16_max(float v) { v = fmaxf(v, dpp<0xB1>(v)); v = fmaxf(v, dpp<0x4E>(v)); v = fmaxf(v, dpp<0x141>(v)); v = fmaxf(v, dpp<0x140>(v)); return v; }
DI float rdlane(float v, int l) { return __builtin_bit_cast(float, __builtin_amdgcn_readlane(__builtin_bit_cast(int, v), l)); }
DI void x32_both(float v, float& lo, float& hi) {
  unsigned a = __builtin_bit_cast(unsigned, v), b = a;
  asm volatile("" : "+v"(b));
  auto r = __builtin_amdgcn_permlane32_swap(a, b, false, false);
  lo = __builtin_bit_cast(float, (unsigned)r[0]); hi = __builtin_bit_cast(float, (unsigned)r[1]);
}
DI void x16_both(float v, float& lo, float& hi) {
  unsigned a = __builtin_bit_cast(unsigned, v), b = a;
  asm volatile("" : "+v"(b));
  auto r = __builtin_amdgcn_permlane16_swap(a, b, false, false);
  lo = __builtin_bit_cast(float, (unsigned)r[0]); hi = __builtin_bit_cast(float, (unsigned)r[1]);
}
DI float x32_sum(float v) { float a, b; x32_both(v, a, b); return a + b; }
DI float x32_max(float v) { float a, b; x32_both(v, a, b); return fmaxf(a, b); }
DI float x16_sum(float v) { float a, b; x16_both(v, a, b); return a + b; }
DI float x16_max(float v) { float a, b; x16_both(v, a, b); return fmaxf(a, b); }
DI v4u ldg(const u16* p) { return __builtin_nontemporal_load((const GAS v4u*)p); }
DI v4u ldx(const u16* p) { return *(const GAS v4u*)p; }
DI v4u ld_sc1(__amdgpu_buffer_rsrc_t rs, int off) { return __builtin_amdgcn_raw_buffer_load_b128(rs, off, 0, 16); }
DI void st_sc1(v4u v, __amdgpu_buffer_rsrc_t rs, int off) { __builtin_amdgcn_raw_buffer_store_b128(v, rs, off, 0, 16); }
#define BAR() do { asm volatile("s_waitcnt lgkmcnt(0)" ::: "memory"); __builtin_amdgcn_s_barrier(); asm volatile("" ::: "memory"); } while (0)
#define PIN() do { asm volatile("" ::: "memory"); __builtin_amdgcn_sched_barrier(0); } while (0)
#define VM0() asm volatile("s_waitcnt vmcnt(0)" ::: "memory")
DI v4u lds_r128(LAS char* L, int off) { return *(LAS v4u*)(L + off); }
DI f32x4 lds_rf4(LAS char* L, int off) { return *(LAS f32x4*)(L + off); }
DI void lds_wf4(LAS char* L, int off, f32x4 v) { *(LAS f32x4*)(L + off) = v; }

DI void transpose8(const v4u (&r)[8], v4u (&t)[8]) {
#pragma unroll
  for (int i = 0; i < 8; ++i)
#pragma unroll
    for (int p = 0; p < 4; ++p)
      t[i][p] = __builtin_amdgcn_perm(r[2 * p + 1][i >> 1], r[2 * p][i >> 1], (i & 1) ? 0x07060302u : 0x05040100u);
}
DI void gemv8_store(v4u a, const v4u (&t)[8], LAS char* L, int off, bool valid) {
  f32x4 d[8];
#pragma unroll
  for (int i = 0; i < 8; ++i) d[i] = mfma16(a, t[i], f32x4{0.f, 0.f, 0.f, 0.f});
  if (valid) {
#pragma unroll
    for (int i = 0; i < 8; ++i) lds_wf4(L, off + i * 16, d[i]);
  }
}
DI int e8(int e) { return e * 8 + (e >> 4) * 16; }
DI v4u tag2(f32x2 v, unsigned ep) { return v4u{__float_as_uint(v.x), ep, __float_as_uint(v.y), ep}; }

struct Ln { float mb0, mb1, mb2, mb3, d0, d1, d2, d3; };
DI Ln ln_stats(const v4u (&xs)[4]) {
  float s = 0.f;
#pragma unroll
  for (int k = 0; k < 4; ++k)
#pragma unroll
    for (int e = 0; e < 4; ++e) s += bf_lo(xs[k][e]) + bf_hi(xs[k][e]);
  const float mean = row16_sum(s) * (1.f / 512.f);
  float q = 0.f;
#pragma unroll
  for (int k = 0; k < 4; ++k)
#pragma unroll
    for (int e = 0; e < 4; ++e) { const float a = bf_lo(xs[k][e]) - mean, b = bf_hi(xs[k][e]) - mean; q += a * a; q += b * b; }
  const float var = rbf(row16_sum(q) * (1.f / 512.f));
  const float mb = rbf(mean), den = 1.f / rbf(sqrtf(rbf(var + 1e-5f)));
  Ln o;
  o.mb0 = rdlane(mb, 0); o.mb1 = rdlane(mb, 16); o.mb2 = rdlane(mb, 32); o.mb3 = rdlane(mb, 48);
  o.d0 = rdlane(den, 0); o.d1 = rdlane(den, 16); o.d2 = rdlane(den, 32); o.d3 = rdlane(den, 48);
  return o;
}
DI float sel4(int b, float a0, float a1, float a2, float a3) { const float lo = (b & 1) ? a1 : a0, hi = (b & 1) ? a3 : a2; return (b & 2) ? hi : lo; }
DI float xn1(float x, float mb, float rden) { return rbf(rbf(x - mb) * rden); }
DI v4u xn_frag(v4u x, float mb, float rden) {
  v4u o;
#pragma unroll
  for (int e = 0; e < 4; ++e) o[e] = pk_bf16(rbf(bf_lo(x[e]) - mb) * rden, rbf(bf_hi(x[e]) - mb) * rden);
  return o;
}
DI unsigned swz(unsigned row) { return ((row & 3) << 2) | ((row >> 2) & 3); }
DI float silu_f(float z) { return z / (1.f + __expf(-z)); }

#define LCNT_ADD(o) do { asm volatile("s_waitcnt lgkmcnt(0)" ::: "memory"); if (lane == 0) __hip_atomic_fetch_add((LAS unsigned*)(L + (o)), 1u, __ATOMIC_RELAXED, __HIP_MEMORY_SCOPE_WORKGROUP); } while (0)
#define LCNT_WAIT(o, n) do { while (__builtin_amdgcn_readfirstlane(__hip_atomic_load((LAS unsigned*)(L + (o)), __ATOMIC_RELAXED, __HIP_MEMORY_SCOPE_WORKGROUP)) < (n)) {} asm volatile("" ::: "memory"); } while (0)

__global__ __launch_bounds__(512, 2) void mla_moe_kernel(const u16* x, const u16* ckv, const u16* wq, const u16* wuk, const u16* wuv,
    const u16* wo, const u16* wr, const u16* wg, const u16* wu, const u16* wd, u16* out, char* ws) {
  extern __shared__ __attribute__((aligned(16))) char smem[];
  LAS char* L = (LAS char*)smem;
  {
    unsigned long long pc0;
    asm volatile("s_getpc_b64 %0" : "=s"(pc0));
    const unsigned voff = threadIdx.x * 16;
    const unsigned ldst = (unsigned)(size_t)L + 131072 + 8192;
    unsigned keep;
    asm volatile("s_mov_b32 %0, m0\n\ts_mov_b32 m0, %3\n\ts_nop 0\n\t"
                 "global_load_lds_dwordx4 %1, %2\n\t"
                 "global_load_lds_dwordx4 %1, %4\n\t"
                 "global_load_lds_dwordx4 %1, %5\n\t"
                 "s_mov_b32 m0, %0"
                 : "=&s"(keep) : "v"(voff), "s"(pc0 & ~255ull), "s"(ldst), "s"((pc0 & ~255ull) + 8192), "s"((pc0 & ~255ull) + CODE_KB * 1024 - 8192) : "memory");
  }
  const int tid = threadIdx.x, lane = tid & 63, w = __builtin_amdgcn_readfirstlane(tid >> 6);
  const int xcd = blockIdx.x & 7, idx = blockIdx.x >> 3;
  const bool roleW = idx < 16;
  const int j = idx & 15;
  const int g = lane >> 4, l15 = lane & 15;
  const unsigned long long t_start = __builtin_amdgcn_s_memrealtime();
  const __amdgpu_buffer_rsrc_t rs = __builtin_amdgcn_make_buffer_rsrc(ws, 0, WS_TOTAL, 0x00020000);

  if (__builtin_expect(roleW, 1)) {
    constexpr int L_RED = 98304, L_QS = 8192, L_Q2 = 9216, L_CNTW = 10240, L_CCB = 12288, L_RED2 = 16384, L_CX = 26624, L_STG = 32768, L_FRW = 108544, L_WT = 61440, L_WK = 65536;
    const int h = xcd * 4 + (j >> 2), j4 = j & 3, ch = j4 >> 1, dh = j4 & 1, pw = xcd * 16 + j;
    const int fgrp = xcd * 2 + (j >> 3), fkq = j & 7, fi = fgrp * 8 + fkq;
    const bool ffn = fgrp < 14;
    const unsigned ep_v = __hip_atomic_load((GAS unsigned*)(ws + WS_EPOCH), RLX_AGENT);
    unsigned ep;
    unsigned long long t_pub = 0;
    const int ks2 = (lane >> 3) & 1, nb8 = lane & 7;
    v4u wa[8], wb[8];
#define LOAD_WO() _Pragma("unroll") for (int r = 0; r < 8; ++r) wb[r] = ldg(wo + (size_t)(h * 128 + 64 * dh + 32 * ks2 + 8 * g + r) * 512 + 64 * w + 8 * nb8)
    if (__builtin_expect(w >= 4, 1)) {
      const int m0 = 128 * j4 + 32 * (w - 4);
      v4u wt[8], xs[4];
#pragma unroll
      for (int k = 0; k < 4; ++k) xs[k] = ldx(x + g * 512 + l15 * 32 + k * 8);
      const v4u xf = ldx(x + (lane & 3) * 512 + m0 + 8 * g);
      const int k0 = 64 * fkq + 32 * (w & 1);
      const v4u fx = ldx(x + (lane & 3) * 512 + k0 + 8 * g);
#pragma unroll
      for (int r = 0; r < 8; ++r) wt[r] = ldg(wq + (size_t)(m0 + 8 * g + r) * 4096 + h * 128 + 8 * l15);
      PIN();
      const Ln ln = ln_stats(xs);
      ep = __builtin_amdgcn_readfirstlane(ep_v) + 1u;
      const int rb = lane & 3;
      const float mb = sel4(rb, ln.mb0, ln.mb1, ln.mb2, ln.mb3), den = sel4(rb, ln.d0, ln.d1, ln.d2, ln.d3);
      const v4u a = xn_frag(xf, mb, den), a0 = xn_frag(fx, mb, den);
      PIN();
      v4u t[8];
      transpose8(wt, t);
      gemv8_store(a, t, L, L_RED + (w - 4) * 2304 + lane * 144, lane < 16);
      BAR();
      BAR();
      BAR();
      if (ffn) {
        const u16* wsrc = (w < 6 ? wg : wu) + (size_t)(k0 + 8 * g) * 1792 + 128 * fgrp + 8 * l15;
        v4u wf[8];
        __builtin_amdgcn_s_sleep(3);
#pragma unroll
        for (int r = 0; r < 8; ++r) wf[r] = ldg(wsrc + (size_t)r * 1792);
        PIN();
        LOAD_WO();
        PIN();
        transpose8(wf, t);
        gemv8_store(a0, t, L, L_FRW + (w - 4) * 2304 + lane * 144, lane < 16);
        LCNT_ADD(L_CNTW);
        LCNT_WAIT(L_CNTW, 4u);
#pragma unroll
        for (int k = 0; k < 2; ++k) {
          const int mat = (w - 4) >> 1, e = (2 * (w & 1) + k) * 64 + lane;
          const f32x2 v = *(LAS f32x2*)(L + L_FRW + (2 * mat) * 2304 + e8(e)) + *(LAS f32x2*)(L + L_FRW + (2 * mat + 1) * 2304 + e8(e));
          st_sc1(tag2(v, ep), rs, WS_F1 + ((fi * 2 + mat) * 256 + e) * 16);
        }
        PIN();
      } else {
        LOAD_WO();
        PIN();
      }
    } else {
      const int c0 = 128 * j4 + 32 * w;
      if (tid == 0) *(LAS v2u*)(L + L_CNTW) = v2u{0u, 0u};
      ep = __builtin_amdgcn_readfirstlane(ep_v) + 1u;
      PIN();
#pragma unroll
      for (int i = 0; i < 8; ++i) {
        const int row = 4 * i + g;
        __builtin_amdgcn_global_load_lds((const GAS unsigned*)(wuk + (size_t)(c0 + row) * 4096 + h * 128 + (l15 ^ (row & 15)) * 8),
                                         (LAS unsigned*)(L + L_WK + w * 8192 + i * 1024), 16, 0, 2);
      }
      PIN();
      BAR();
      {
        f32x2 s = {0.f, 0.f};
#pragma unroll
        for (int k = 0; k < 4; ++k) s += *(LAS f32x2*)(L + L_RED + k * 2304 + e8(w * 64 + lane));
        st_sc1(tag2(s, ep), rs, WS_H0 + (((h * 4 + j4) * 4 + w) * 64 + lane) * 16);
        if (tid == 0) st_sc1(v4u{ep, (unsigned)__builtin_amdgcn_s_memrealtime(), ep, 0u}, rs, WS_HINT1 + pw * 128);
      }
      PIN();
      BAR();
      asm volatile("s_waitcnt vmcnt(2)" ::: "memory");
      if (w) asm volatile("s_waitcnt vmcnt(1)" ::: "memory");
      v4u wk[8];
#pragma unroll
      for (int rt = 0; rt < 2; ++rt)
#pragma unroll
        for (int ks = 0; ks < 4; ++ks) wk[rt * 4 + ks] = lds_r128(L, L_WK + w * 8192 + (16 * rt + l15) * 256 + 16 * ((4 * ks + g) ^ l15));
      __builtin_amdgcn_s_sleep(H0_DELAY);
      v4u v[4];
      for (unsigned spins = 0;; ++spins) {
        asm volatile("" ::: "memory");
        unsigned bad = 0u;
#pragma unroll
        for (int mq = 0; mq < 4; ++mq) v[mq] = ld_sc1(rs, WS_H0 + (((h * 4 + mq) * 4 + w) * 64 + lane) * 16);
#pragma unroll
        for (int mq = 0; mq < 4; ++mq) bad |= (v[mq].y ^ ep) | (v[mq].w ^ ep);
        if (__all(bad == 0u) || spins > SPIN_CAP) break;
        __builtin_amdgcn_s_sleep(2);
      }
      const float q0 = (__uint_as_float(v[0].x) + __uint_as_float(v[1].x)) + (__uint_as_float(v[2].x) + __uint_as_float(v[3].x));
      const float q1 = (__uint_as_float(v[0].z) + __uint_as_float(v[1].z)) + (__uint_as_float(v[2].z) + __uint_as_float(v[3].z));
      {
        const unsigned qq = pk_bf16(q0, q1);
        *(LAS u16*)(L + L_QS + ((2 * (lane & 1)) * 128 + 32 * w + (lane >> 1)) * 2) = (u16)qq;
        *(LAS u16*)(L + L_QS + ((2 * (lane & 1) + 1) * 128 + 32 * w + (lane >> 1)) * 2) = (u16)(qq >> 16);
      }
      LCNT_ADD(L_CNTW + 4);
      LCNT_WAIT(L_CNTW + 4, 4u);
      v4u bq[4];
#pragma unroll
      for (int ks = 0; ks < 4; ++ks) bq[ks] = lds_r128(L, L_QS + ((lane & 3) * 128 + 32 * ks + 8 * g) * 2);
#pragma unroll
      for (int rt = 0; rt < 2; ++rt) {
        f32x4 d = {0.f, 0.f, 0.f, 0.f};
#pragma unroll
        for (int ks = 0; ks < 4; ++ks) d = mfma16(wk[rt * 4 + ks], bq[ks], d);
        if (l15 < 4) *(LAS v2u*)(L + L_Q2 + w * 256 + l15 * 64 + (4 * rt + g) * 8) = v2u{pk_bf16(d[0], d[1]), pk_bf16(d[2], d[3])};
      }
      asm volatile("s_waitcnt lgkmcnt(0)" ::: "memory");
      if (lane < 32) {
        const v2u d = *(LAS v2u*)(L + L_Q2 + w * 256 + lane * 8);
        st_sc1(v4u{d.x, ep, d.y, ep}, rs, WS_H1 + (((lane >> 3) * 32 + h) * 128 + (c0 >> 2) + (lane & 7)) * 16);
      }
      t_pub = __builtin_amdgcn_s_memrealtime();
      PIN();
      BAR();
#pragma unroll
      for (int r = 0; r < 8; ++r) wa[r] = ldg(wuv + (size_t)(256 * ch + 64 * w + 32 * ks2 + 8 * g + r) * 4096 + h * 128 + 64 * dh + 8 * nb8);
      LOAD_WO();
      PIN();
    }
    v4u wvT[8], woT[8];
    {
      if (w < 4) transpose8(wa, wvT);
      transpose8(wb, woT);
#pragma unroll
      for (int i = 0; i < 8; ++i) asm volatile("" : "+v"(woT[i]), "+v"(wvT[i]));
    }
    if (w < 4) {
      const int bb = w;
      {
        v4u t1;
        while (__builtin_amdgcn_s_memrealtime() - t_pub < H2_GATE) __builtin_amdgcn_s_sleep(8);
        for (unsigned spins = 0; spins <= SPIN_CAP; ++spins) {
          asm volatile("" ::: "memory");
          t1 = ld_sc1(rs, WS_HINT2 + (bb * 32 + (lane & 31)) * 128);
          if (__all((t1.x == ep) & (t1.z == ep))) break;
          __builtin_amdgcn_s_sleep(POLL_SLEEP);
        }
        while (__any((unsigned)__builtin_amdgcn_s_memrealtime() - t1.y < H2_WAIT)) __builtin_amdgcn_s_sleep(1);
      }
      const int hf = lane >> 5, l31 = lane & 31;
      v4u p[16], s;
      const int base = WS_H2P + (((bb * 32 + hf) * 32 + h) * 512 + 256 * ch + 8 * l31) * 2;
      for (unsigned spins = 0;; ++spins) {
        asm volatile("" ::: "memory");
        s = ld_sc1(rs, WS_H2S + ((bb * 32 + l31) * 32 + h) * 16);
#pragma unroll
        for (int i = 0; i < 16; ++i) p[i] = ld_sc1(rs, base + (i ^ (8 * dh)) * (2 * 32 * 512 * 2));
        unsigned mx = 0u;
#pragma unroll
        for (int i = 0; i < 16; ++i) mx = max(max(mx, max(p[i].x, p[i].y)), max(p[i].z, p[i].w));
        if (__all((s.y == ep) & (s.w == ep) & (mx != SENT)) || spins > SPIN_CAP) break;
        __builtin_amdgcn_s_sleep(2);
      }
      if (tid == 0) st_sc1(v4u{ep, (unsigned)__builtin_amdgcn_s_memrealtime(), ep, 0u}, rs, WS_HINT3 + pw * 128);
      f32x4 wv[4];
      {
        const float m = __uint_as_float(s.x), l = __uint_as_float(s.z);
        const float Mx = x16_max(row16_max(m));
        const float e = __builtin_amdgcn_exp2f(m - Mx);
        const float wgt = e * __builtin_amdgcn_rcpf(x16_sum(row16_sum(e * l)));
        *(LAS float*)(L + L_WT + w * 128 + (lane & 1) * 64 + (((l31 >> 1) ^ (8 * dh)) * 4)) = wgt;
        asm volatile("" ::: "memory");
#pragma unroll
        for (int k = 0; k < 4; ++k) wv[k] = lds_rf4(L, L_WT + w * 128 + hf * 64 + k * 16);
      }
      f32x2 acc[4] = {{0.f, 0.f}, {0.f, 0.f}, {0.f, 0.f}, {0.f, 0.f}};
#pragma unroll
      for (int i = 0; i < 16; ++i) {
        const float ww = wv[i >> 2][i & 3];
#pragma unroll
        for (int e = 0; e < 4; ++e) acc[e] += f32x2{ww, ww} * f32x2{bf_lo(p[i][e]), bf_hi(p[i][e])};
      }
      f32x2 r2[2];
#pragma unroll
      for (int e = 0; e < 2; ++e)
#pragma unroll
        for (int k = 0; k < 2; ++k) {
          auto r = __builtin_amdgcn_permlane32_swap(__float_as_uint(acc[e][k]), __float_as_uint(acc[e + 2][k]), false, false);
          r2[e][k] = __uint_as_float(r[0]) + __uint_as_float(r[1]);
        }
      *(LAS v2u*)(L + L_CCB + bb * 576 + (8 * l31 + 4 * hf) * 2) = v2u{pk_bf16(r2[0].x, r2[0].y), pk_bf16(r2[1].x, r2[1].y)};
    }
    BAR();
    const int rb = lane & 3, rks = (lane >> 2) & 1;
    const bool dvalid = g == ks2;
    if (w < 4) {
      const v4u a = lds_r128(L, L_CCB + rb * 576 + (64 * w + 32 * rks + 8 * g) * 2);
      gemv8_store(a, wvT, L, L_RED2 + (w * 2 + ks2) * 1152 + nb8 * 144, dvalid);
    }
    BAR();
    {
      f32x4 a0 = {0.f, 0.f, 0.f, 0.f};
#pragma unroll
      for (int p = 0; p < 8; ++p) a0 += lds_rf4(L, L_RED2 + p * 1152 + e8(2 * lane));
      {
        const unsigned c01 = pk_bf16(a0[0], a0[1]), c23 = pk_bf16(a0[2], a0[3]);
        *(LAS u16*)(L + L_CX + w * 512 + lane * 2) = (u16)c01;
        *(LAS u16*)(L + L_CX + w * 512 + 128 + lane * 2) = (u16)(c01 >> 16);
        *(LAS u16*)(L + L_CX + w * 512 + 256 + lane * 2) = (u16)c23;
        *(LAS u16*)(L + L_CX + w * 512 + 384 + lane * 2) = (u16)(c23 >> 16);
      }
      asm volatile("" ::: "memory");
      const v4u a = lds_r128(L, L_CX + w * 512 + rb * 128 + (32 * rks + 8 * g) * 2);
      gemv8_store(a, woT, L, L_STG + (w * 2 + ks2) * 1152 + nb8 * 144, dvalid);
      asm volatile("" ::: "memory");
#pragma unroll
      for (int k = 0; k < 2; ++k) {
        const f32x2 v = *(LAS f32x2*)(L + L_STG + w * 2304 + e8(64 * k + lane)) + *(LAS f32x2*)(L + L_STG + w * 2304 + 1152 + e8(64 * k + lane));
        st_sc1(tag2(v, ep), rs, WS_H3S + (pw * 1024 + 128 * w + 64 * k + lane) * 16);
      }
    }
    return;
  }
  {
    const int b = xcd >> 1, slice = (xcd & 1) * 16 + j;
    const int fgrp = xcd * 2 + (j >> 3), fkq = j & 7, fi = fgrp * 8 + fkq;
    const bool ffn = fgrp < 14;
    unsigned ep_v;
    asm volatile("global_load_dword %0, %1, off sc1" : "=v"(ep_v) : "v"(ws + WS_EPOCH) : "memory");
    PIN();
    {
      const u16* ksrc = ckv + ((size_t)(b * 4096 + slice * 128 + 16 * w)) * 512 + g * 128;
#pragma unroll
      for (int i = 0; i < 16; ++i) {
        if (i == A_FIRST) { PIN(); while (__builtin_amdgcn_s_memrealtime() - t_start < A_SECOND) __builtin_amdgcn_s_sleep(1); PIN(); }
        __builtin_amdgcn_global_load_lds((const GAS unsigned*)(ksrc + (size_t)i * 512 + (l15 ^ swz(i)) * 8),
                                         (LAS unsigned*)(L + (16 * w + i) * 1024), 16, 0, 2);
      }
    }
    PIN();
    const int pofs = WS_H2P + (((b * 32 + slice) * 32 + (lane >> 3)) * 512 + 64 * w + 8 * (lane & 7)) * 2;
    asm volatile("s_waitcnt vmcnt(0)" : "+v"(ep_v) :: "memory");
    const unsigned ep = __builtin_amdgcn_readfirstlane(ep_v) + 1u;
    v4u ka[16];
    v2u tr[4][4][2];
    const unsigned lb = (unsigned)(size_t)L;
    {
      const int krow = 8 * ((l15 >> 2) & 1) + 4 * (l15 >> 3) + (l15 & 3);
      const int sk = swz(krow);
#pragma unroll
      for (int k4 = 0; k4 < 4; ++k4) {
        const unsigned addr = lb + (16 * w + krow) * 1024 + 16 * ((4 * k4 + g) ^ sk);
#pragma unroll
        for (int ct = 0; ct < 4; ++ct) asm volatile("ds_read_b128 %0, %1 offset:%2" : "=v"(ka[ct * 4 + k4]) : "v"(addr), "i"(ct * 256) : "memory");
      }
    }
    BAR();
    {
      const int q4 = l15 >> 2, p4 = lane & 3;
      const unsigned row = 8 * (g & 1) + 4 * (g >> 1) + q4;
#pragma unroll
      for (int u = 0; u < 2; ++u) {
        const unsigned addr = lb + row * 1024 + (w >> 1) * 256 + 16 * ((8 * (w & 1) + 4 * u + p4) ^ swz(row));
#pragma unroll
        for (int s = 0; s < 4; ++s)
#pragma unroll
          for (int t2 = 0; t2 < 2; ++t2)
#pragma unroll
            for (int e = 0; e < 2; ++e)
              asm volatile("ds_read_b64_tr_b16 %0, %1 offset:%2" : "=v"(tr[s][u * 2 + e][t2]) : "v"(addr + (s >> 1) * 65536), "i"((s & 1) * 32768 + t2 * 16384 + 8 * e) : "memory");
      }
    }
    {
      const int qbase = WS_H1 + ((b * 32 + 4 * w) * 128 + lane) * 16;
      while (__builtin_amdgcn_s_memrealtime() - t_start < H1_GATE) __builtin_amdgcn_s_sleep(4);
      {
        v4u t1;
        for (unsigned spins = 0; spins <= SPIN_CAP; ++spins) {
          asm volatile("" ::: "memory");
          t1 = ld_sc1(rs, WS_HINT1 + (16 * w + l15) * 128);
          if (__all((t1.x == ep) & (t1.z == ep))) break;
          __builtin_amdgcn_s_sleep(POLL_SLEEP);
        }
        while (__any((unsigned)__builtin_amdgcn_s_memrealtime() - t1.y < H1_WAIT)) __builtin_amdgcn_s_sleep(1);
      }
      v4u qv[8];
      for (unsigned spins = 0;; ++spins) {
        asm volatile("" ::: "memory");
#pragma unroll
        for (int i = 0; i < 8; ++i) qv[i] = ld_sc1(rs, qbase + i * 1024);
        unsigned bad = 0u;
#pragma unroll
        for (int i = 0; i < 8; ++i) bad |= (qv[i].y ^ ep) | (qv[i].w ^ ep);
        if (__all(bad == 0u) || spins > SPIN_CAP) break;
        __builtin_amdgcn_s_sleep(2);
      }
      const int gq = (lane >> 1) & 3;
#pragma unroll
      for (int i = 0; i < 8; ++i) {
        const int h16 = (4 * w + (i >> 1)) & 15, ks = 8 * (i & 1) + (lane >> 3);
        *(LAS v2u*)(L + L_QA + (w >> 2) * 16384 + ks * 1024 + ((h16 ^ (gq | ((ks & 1) << 2))) + 16 * gq) * 16 + 8 * (lane & 1)) = v2u{qv[i].x, qv[i].z};
      }
    }
#pragma unroll
    for (int i = 0; i < 4; ++i) st_sc1(v4u{SENT, SENT, SENT, SENT}, rs, pofs + i * 8192);
    BAR();
#pragma unroll
    for (int s = 0; s < 4; ++s) {
      asm volatile("" : "+v"(tr[s][0][0]), "+v"(tr[s][0][1]), "+v"(tr[s][1][0]), "+v"(tr[s][1][1]), "+v"(tr[s][2][0]), "+v"(tr[s][2][1]), "+v"(tr[s][3][0]), "+v"(tr[s][3][1]));
      asm volatile("" : "+v"(ka[4 * s]), "+v"(ka[4 * s + 1]), "+v"(ka[4 * s + 2]), "+v"(ka[4 * s + 3]));
    }
    float sc[2][4];
    {
      constexpr int RING = 16;
      v4u bq[RING];
      const unsigned qa0 = (unsigned)(size_t)L + L_QA + ((l15 ^ g) + 16 * g) * 16, qa1 = (unsigned)(size_t)L + L_QA + ((l15 ^ (g | 4)) + 16 * g) * 16;
#define QREAD(i) asm volatile("ds_read_b128 %0, %1 offset:%2" : "=v"(bq[(i) % RING]) : "v"((((i) >> 1) & 1) ? qa1 : qa0), "i"(((i) & 1) * 16384 + ((i) >> 1) * 1024) : "memory")
#pragma unroll
      for (int i = 0; i < RING; ++i) QREAD(i);
      f32x4 acc[2] = {{0.f, 0.f, 0.f, 0.f}, {0.f, 0.f, 0.f, 0.f}};
#pragma unroll
      for (int i = 0; i < 32; i += 2) {
        asm volatile("s_waitcnt lgkmcnt(%2)" : "+v"(bq[i % RING]), "+v"(bq[(i + 1) % RING]) : "i"(i + RING <= 32 ? RING - 2 : 30 - i) : "memory");
        acc[0] = mfma16(ka[i >> 1], bq[i % RING], acc[0]);
        acc[1] = mfma16(ka[i >> 1], bq[(i + 1) % RING], acc[1]);
        __builtin_amdgcn_sched_barrier(0);
        if (i + RING < 32) { QREAD(i + RING); QREAD(i + RING + 1); }
      }
      float m[2];
#pragma unroll
      for (int hh = 0; hh < 2; ++hh) {
#pragma unroll
        for (int e = 0; e < 4; ++e) sc[hh][e] = acc[hh][e] * (0.08838834764831845f * 1.4426950408889634f);
        m[hh] = fmaxf(fmaxf(sc[hh][0], sc[hh][1]), fmaxf(sc[hh][2], sc[hh][3]));
      }
      auto r1 = __builtin_amdgcn_permlane16_swap(__float_as_uint(m[0]), __float_as_uint(m[1]), false, false);
      const float mm = x32_max(fmaxf(__uint_as_float(r1[0]), __uint_as_float(r1[1])));
      if (lane < 32) *(LAS float*)(L + L_MX + (lane * 8 + w) * 4) = mm;
    }
    BAR();
    float M[2];
    {
      f32x4 mx[2][2];
#pragma unroll
      for (int hh = 0; hh < 2; ++hh) { mx[hh][0] = lds_rf4(L, L_MX + (hh * 16 + l15) * 32); mx[hh][1] = lds_rf4(L, L_MX + (hh * 16 + l15) * 32 + 16); }
#pragma unroll
      for (int hh = 0; hh < 2; ++hh) {
        M[hh] = fmaxf(fmaxf(fmaxf(mx[hh][0][0], mx[hh][0][1]), fmaxf(mx[hh][0][2], mx[hh][0][3])), fmaxf(fmaxf(mx[hh][1][0], mx[hh][1][1]), fmaxf(mx[hh][1][2], mx[hh][1][3])));
        const v2u p = {pk_bf16(__builtin_amdgcn_exp2f(sc[hh][0] - M[hh]), __builtin_amdgcn_exp2f(sc[hh][1] - M[hh])),
                       pk_bf16(__builtin_amdgcn_exp2f(sc[hh][2] - M[hh]), __builtin_amdgcn_exp2f(sc[hh][3] - M[hh]))};
        *(LAS v2u*)(L + L_P + hh * 4096 + ((w >> 1) * 64 + lane) * 16 + 8 * (w & 1)) = p;
      }
    }
    VM0();
    BAR();
    if (tid == 64) st_sc1(v4u{ep, (unsigned)__builtin_amdgcn_s_memrealtime(), ep, 0u}, rs, WS_HINT2 + (b * 32 + slice) * 128);
    {
      v4u pb[2][4];
#pragma unroll
      for (int hh = 0; hh < 2; ++hh)
#pragma unroll
        for (int s = 0; s < 4; ++s) pb[hh][s] = lds_r128(L, L_P + hh * 4096 + (s * 64 + lane) * 16);
      if (w == 0) {
        const v4u ones = {0x3F803F80u, 0x3F803F80u, 0x3F803F80u, 0x3F803F80u};
        f32x4 l0 = {0.f, 0.f, 0.f, 0.f}, l1 = {0.f, 0.f, 0.f, 0.f};
#pragma unroll
        for (int s = 0; s < 4; ++s) { l0 = mfma16(ones, pb[0][s], l0); l1 = mfma16(ones, pb[1][s], l1); }
        if (lane < 32)
          st_sc1(v4u{__float_as_uint(lane < 16 ? M[0] : M[1]), ep, __float_as_uint(lane < 16 ? l0[0] : l1[0]), ep}, rs, WS_H2S + ((b * 32 + slice) * 32 + lane) * 16);
      }
#pragma unroll
      for (int hh = 0; hh < 2; ++hh) {
        f32x4 o[4];
#pragma unroll
        for (int c = 0; c < 4; ++c) o[c] = f32x4{0.f, 0.f, 0.f, 0.f};
#pragma unroll
        for (int s = 0; s < 4; ++s)
#pragma unroll
          for (int c = 0; c < 4; ++c) o[c] = mfma16(v4u{tr[s][c][0].x, tr[s][c][0].y, tr[s][c][1].x, tr[s][c][1].y}, pb[hh][s], o[c]);
#pragma unroll
        for (int u = 0; u < 2; ++u) {
          v4u v = {pk_bf16(o[2 * u][0], o[2 * u][1]), pk_bf16(o[2 * u][2], o[2 * u][3]), pk_bf16(o[2 * u + 1][0], o[2 * u + 1][1]), pk_bf16(o[2 * u + 1][2], o[2 * u + 1][3])};
#pragma unroll
          for (int e = 0; e < 4; ++e) v[e] = min(v[e], SENT - 1u);
          *(LAS v4u*)(L + L_OST + w * 4096 + (hh * 16 + l15) * 128 + 16 * ((4 * u + g) ^ (l15 & 7))) = v;
        }
        asm volatile("" ::: "memory");
#pragma unroll
        for (int i = 2 * hh; i < 2 * hh + 2; ++i) {
          const int hr = 8 * i + (lane >> 3);
          st_sc1(lds_r128(L, L_OST + w * 4096 + hr * 128 + 16 * ((lane & 7) ^ (hr & 7))), rs, pofs + i * 8192);
        }
      }
    }
    PIN();
    __builtin_amdgcn_s_barrier();
    const unsigned long long t_s3 = __builtin_amdgcn_s_memrealtime();
    if (ffn && (w == 1 || w == 2)) {
      v4u wf[8];
      const int wd2 = w - 1;
#pragma unroll
      for (int r = 0; r < 8; ++r) wf[r] = ldg(wd + (size_t)(16 * fi + 8 * (g & 1) + r) * 512 + 256 * wd2 + 128 * (g >> 1) + 8 * l15);
      v4u gv[8];
      const int gofs = WS_F1 + ((fgrp * 16 + (lane >> 5)) * 256 + 32 * fkq + (lane & 31)) * 16;
      for (unsigned spins = 0;; ++spins) {
        asm volatile("" ::: "memory");
        bool ok = true;
#pragma unroll
        for (int k = 0; k < 8; ++k) gv[k] = ld_sc1(rs, gofs + k * 8192);
#pragma unroll
        for (int k = 0; k < 8; ++k) ok &= (gv[k].y == ep) & (gv[k].w == ep);
        if (__all(ok) || spins > SPIN_CAP) break;
        __builtin_amdgcn_s_sleep(8);
      }
      float s0 = 0.f, s1 = 0.f;
#pragma unroll
      for (int k = 0; k < 8; ++k) { s0 += __uint_as_float(gv[k].x); s1 += __uint_as_float(gv[k].z); }
      s0 = rbf(s0); s1 = rbf(s1);
      float g0, u0, g1, u1;
      x32_both(s0, g0, u0);
      x32_both(s1, g1, u1);
      if (lane < 32) {
        const unsigned hh2 = pk_bf16(rbf(silu_f(g0)) * u0, rbf(silu_f(g1)) * u1);
        *(LAS u16*)(L + L_HM + wd2 * 128 + ((2 * (lane & 1)) * 16 + (lane >> 1)) * 2) = (u16)hh2;
        *(LAS u16*)(L + L_HM + wd2 * 128 + ((2 * (lane & 1) + 1) * 16 + (lane >> 1)) * 2) = (u16)(hh2 >> 16);
      }
      asm volatile("s_waitcnt lgkmcnt(0)" ::: "memory");
      v4u a = lds_r128(L, L_HM + wd2 * 128 + ((lane & 3) * 16 + 8 * (g & 1)) * 2);
      if ((g >> 1) != ((lane >> 2) & 1)) a = v4u{0u, 0u, 0u, 0u};
      v4u t[8];
      transpose8(wf, t);
      const int sb = L_FS + wd2 * 4608;
      gemv8_store(a, t, L, sb + lane * 144, g < 2);
      asm volatile("s_waitcnt lgkmcnt(0)" ::: "memory");
#pragma unroll
      for (int k = 0; k < 8; ++k) {
        const f32x2 v = *(LAS f32x2*)(L + sb + e8(64 * k + lane));
        st_sc1(tag2(v, ep), rs, WS_H3Y + (fi * 1024 + 512 * wd2 + 64 * k + lane) * 16);
      }
    }
    const int aj = xcd * 16 + j;
    float rs0 = 0.f, rs1 = 0.f, xa = 0.f, xb = 0.f, xna = 0.f, xnb = 0.f;
    if (w == 0) {
      v4u wrv[4], xs[4];
#pragma unroll
      for (int k = 0; k < 4; ++k) xs[k] = ldx(x + g * 512 + l15 * 32 + k * 8);
#pragma unroll
      for (int k = 0; k < 4; ++k) wrv[k] = ldx(wr + l15 * 32 + k * 8);
      const int b0 = 2 * (lane & 1), mm = 4 * aj + ((lane >> 1) & 3);
      xa = bf_lo(*(const GAS u16*)(x + b0 * 512 + mm));
      xb = bf_lo(*(const GAS u16*)(x + (b0 + 1) * 512 + mm));
      const Ln ln = ln_stats(xs);
      const float mb = sel4(g, ln.mb0, ln.mb1, ln.mb2, ln.mb3), den = sel4(g, ln.d0, ln.d1, ln.d2, ln.d3);
      float r = 0.f;
#pragma unroll
      for (int k = 0; k < 4; ++k)
#pragma unroll
        for (int e = 0; e < 4; ++e) { r += xn1(bf_lo(xs[k][e]), mb, den) * bf_lo(wrv[k][e]); r += xn1(bf_hi(xs[k][e]), mb, den) * bf_hi(wrv[k][e]); }
      r = rbf(row16_sum(r));
      r = rbf(1.f / (1.f + __expf(-r)));
      rs0 = (lane & 1) ? rdlane(r, 32) : rdlane(r, 0);
      rs1 = (lane & 1) ? rdlane(r, 48) : rdlane(r, 16);
      xna = xn1(xa, (lane & 1) ? ln.mb2 : ln.mb0, (lane & 1) ? ln.d2 : ln.d0);
      xnb = xn1(xb, (lane & 1) ? ln.mb3 : ln.mb1, (lane & 1) ? ln.d3 : ln.d1);
    }
    {
      const int piece = tid >> 3, sub = tid & 7;
      const bool yv = piece < 48;
      const int o0s = WS_H3S + (piece * 1024 + 8 * aj + sub) * 16;
      const int oy = WS_H3Y + (piece * 1024 + 8 * aj + sub) * 16;
      const int oy2 = oy + (yv ? 64 * 16384 : 0);
      while (__builtin_amdgcn_s_memrealtime() - t_s3 < H3_GATE) __builtin_amdgcn_s_sleep(8);
      v4u a, c, y0, y1;
      for (unsigned spins = 0;; ++spins) {
        asm volatile("" ::: "memory");
        y0 = ld_sc1(rs, oy);
        y1 = ld_sc1(rs, oy2);
        if (__all((y0.y == ep) & (y0.w == ep) & (y1.y == ep) & (y1.w == ep)) || spins > SPIN_CAP) break;
        __builtin_amdgcn_s_sleep(POLL_SLEEP);
      }
      {
        f32x2 v = {__uint_as_float(y0.x) + (yv ? __uint_as_float(y1.x) : 0.f), __uint_as_float(y0.z) + (yv ? __uint_as_float(y1.z) : 0.f)};
#pragma unroll
        for (int e = 0; e < 2; ++e) v[e] = x32_sum(x16_sum(v[e] + dpp<0x128>(v[e])));
        if (lane < 8) *(LAS f32x2*)(L + L_SR + 512 + (w * 8 + lane) * 8) = v;
      }
      BAR();
      float ga = 0.f, gb = 0.f;
      if (w == 0) {
        f32x2 t = {0.f, 0.f};
#pragma unroll
        for (int k = 0; k < 8; ++k) t += *(LAS f32x2*)(L + L_SR + 512 + (k * 8 + (lane & 7)) * 8);
        ga = rbf(silu_f(rbf(xna + rbf(rbf(t.x) * rs0))));
        gb = rbf(silu_f(rbf(xnb + rbf(rbf(t.y) * rs1))));
      }
      {
        v4u t1;
        for (unsigned spins = 0; spins <= SPIN_CAP; ++spins) {
          asm volatile("" ::: "memory");
          t1 = ld_sc1(rs, WS_HINT3 + (8 * w + (lane & 7) + 64 * ((lane >> 3) & 1)) * 128);
          if (__all((t1.x == ep) & (t1.z == ep))) break;
          __builtin_amdgcn_s_sleep(POLL_SLEEP);
        }
        while (__any((unsigned)__builtin_amdgcn_s_memrealtime() - t1.y < H3_WAIT)) __builtin_amdgcn_s_sleep(1);
      }
      for (unsigned spins = 0;; ++spins) {
        asm volatile("" ::: "memory");
        a = ld_sc1(rs, o0s);
        c = ld_sc1(rs, o0s + 64 * 16384);
        if (__all(((a.y ^ ep) | (a.w ^ ep) | (c.y ^ ep) | (c.w ^ ep)) == 0u) || spins > SPIN_CAP) break;
        __builtin_amdgcn_s_sleep(2);
      }
      f32x2 v = {__uint_as_float(a.x) + __uint_as_float(c.x), __uint_as_float(a.z) + __uint_as_float(c.z)};
#pragma unroll
      for (int e = 0; e < 2; ++e) v[e] = x32_sum(x16_sum(v[e] + dpp<0x128>(v[e])));
      if (lane < 8) *(LAS f32x2*)(L + L_SR + (w * 8 + lane) * 8) = v;
      BAR();
      if (w == 0) {
        f32x2 t = {0.f, 0.f};
#pragma unroll
        for (int k = 0; k < 8; ++k) t += *(LAS f32x2*)(L + L_SR + (k * 8 + (lane & 7)) * 8);
        const unsigned o2 = pk_bf16(xa + rbf(rbf(t.x) * ga), xb + rbf(rbf(t.y) * gb));
        if (lane < 8) {
          const int b0 = 2 * (lane & 1), mm = 4 * aj + (lane >> 1);
          *(GAS u16*)(out + b0 * 512 + mm) = (u16)o2;
          *(GAS u16*)(out + (b0 + 1) * 512 + mm) = (u16)(o2 >> 16);
        }
        if (aj == 0 && lane == 0) __hip_atomic_store((GAS unsigned*)(ws + WS_EPOCH), ep, RLX_AGENT);
      }
    }
  }
}

extern "C" void kernel_launch(void* const* d_in, const int* in_sizes, int n_in, void* d_out, int out_size, void* d_ws, size_t ws_size,
                              hipStream_t stream) {
  (void)hipFuncSetAttribute((const void*)mla_moe_kernel, hipFuncAttributeMaxDynamicSharedMemorySize, L_TOTAL);
  mla_moe_kernel<<<256, 512, L_TOTAL, stream>>>((const u16*)d_in[0], (const u16*)d_in[1], (const u16*)d_in[2], (const u16*)d_in[3],
                                                (const u16*)d_in[4], (const u16*)d_in[5], (const u16*)d_in[7], (const u16*)d_in[8],
                                                (const u16*)d_in[9], (const u16*)d_in[10], (u16*)d_out, (char*)d_ws);
}
```
